```python
import torch

def _softmax(x, axis=-1):
    return torch.softmax(x, dim=axis)
def _gelu(x):
    return torch.nn.functional.gelu(x, approximate="tanh")
def _silu(x):
    return torch.nn.functional.silu(x)
def _sigmoid(x):
    return torch.sigmoid(x)
def _layernorm(x, axis=-1, eps=1e-5):
    m = x.mean(dim=axis, keepdim=True)
    v = x.var(dim=axis, keepdim=True, unbiased=False)
    return (x - m) / torch.sqrt(v + eps)
def _scatter_add(tgt, idx, src, axis=0):
    return tgt.clone().index_add_(axis, idx, src)
def _segment_sum(src, seg, n):
    out = torch.zeros((n,) + src.shape[1:], dtype=src.dtype)
    return out.index_add_(0, seg, src)
def _pad(x, pw):
    flat = tuple(v for pair in reversed(pw) for v in pair)
    return torch.nn.functional.pad(x, flat)

kernel_name = 'interleave_layers__spectral_fnet__glu_gated_glu__prompt'

def setup_inputs():

    _w = (lambda a: (lambda t: t.to(torch.bfloat16) if t.is_floating_point() else t)(torch.as_tensor(a)))
    def _A_setup():
        B, S, D = 1, 2048, 1024
        ns = torch.arange(S)[:, None] * torch.arange(S)[None, :]
        nd = torch.arange(D)[:, None] * torch.arange(D)[None, :]
        Ws_re = torch.cos(-2.0 * torch.pi * ns / S).to(float)
        Ws_im = torch.sin(-2.0 * torch.pi * ns / S).to(float)
        Wd_re = torch.cos(-2.0 * torch.pi * nd / D).to(float)
        Wd_im = torch.sin(-2.0 * torch.pi * nd / D).to(float)
        return {
            "x": _w((lambda *s: torch.randn(*s, dtype=torch.bfloat16) * 0.05)(B, S, D)),
            "dft_seq_re": _w(Ws_re),
            "dft_seq_im": _w(Ws_im),
            "dft_hid_re": _w(Wd_re),
            "dft_hid_im": _w(Wd_im),
        }
    _A_in = _A_setup()

    def _B_setup():
        B, S, D, F = 1, 2048, 1024, 3584
        return {
            "x": _w((lambda *s: torch.randn(*s, dtype=torch.bfloat16) * 0.05)(B, S, D)),
            "w_gate": _w((lambda *s: torch.randn(*s, dtype=torch.bfloat16) * 1.6)(D, F)),
            "w_up": _w((lambda *s: torch.randn(*s, dtype=torch.bfloat16) * 0.8)(D, F)),
            "w_down": _w((lambda *s: torch.randn(*s, dtype=torch.bfloat16) * 0.8)(F, D)),
        }
    _B_in = _B_setup()

    return {
        "A_x": _A_in["x"],
        "A_dft_seq_re": _A_in["dft_seq_re"],
        "A_dft_seq_im": _A_in["dft_seq_im"],
        "A_dft_hid_re": _A_in["dft_hid_re"],
        "A_dft_hid_im": _A_in["dft_hid_im"],
        "B_x": _B_in["x"],
        "B_w_gate": _B_in["w_gate"],
        "B_w_up": _B_in["w_up"],
        "B_w_down": _B_in["w_down"],
    }

def reference(A_x, A_dft_seq_re, A_dft_seq_im, A_dft_hid_re, A_dft_hid_im, B_x, B_w_gate, B_w_up, B_w_down):
    def _A_ref(x, dft_seq_re, dft_seq_im, dft_hid_re, dft_hid_im):
        a = torch.einsum("bsd,ed->bse", x, dft_hid_re)
        b = torch.einsum("bsd,ed->bse", x, dft_hid_im)
        y = torch.einsum("ts,bse->bte", dft_seq_re, a) - torch.einsum("ts,bse->bte", dft_seq_im, b)
        return y

    def _B_ref(x, w_gate, w_up, w_down):
        g = torch.einsum("bsd,df->bsf", x, w_gate)
        u = torch.einsum("bsd,df->bsf", x, w_up)
        h = _sigmoid(g) * u
        return torch.einsum("bsf,fd->bsd", h, w_down)

    x = A_x
    x = x + _A_ref(_layernorm(x, axis=-1), A_dft_seq_re, A_dft_seq_im, A_dft_hid_re, A_dft_hid_im)
    x = x + _B_ref(_layernorm(x, axis=-1), B_w_gate, B_w_up, B_w_down)
    x = x + _A_ref(_layernorm(x, axis=-1), A_dft_seq_re, A_dft_seq_im, A_dft_hid_re, A_dft_hid_im)
    x = x + _B_ref(_layernorm(x, axis=-1), B_w_gate, B_w_up, B_w_down)
    return x
```

```cpp
#include <hip/hip_runtime.h>
#include <hip/hip_bf16.h>
#include <stdint.h>

typedef unsigned short u16;
typedef __attribute__((ext_vector_type(8))) short bf16x8;
typedef __attribute__((ext_vector_type(4))) short bf16x4;
typedef __attribute__((ext_vector_type(4))) float f32x4;
typedef __attribute__((ext_vector_type(4))) unsigned short u16x4;
#define LDS_AS __attribute__((address_space(3)))

constexpr int S_ = 2048, D_ = 1024, F_ = 3584;
constexpr int NPART = 16;
constexpr int CSG = 16;
constexpr float LN_EPS = 1e-5f;

__device__ __forceinline__ float bf2f(u16 h) { return __uint_as_float(((unsigned)h) << 16); }
__device__ __forceinline__ u16 f2bf(float f) { __hip_bfloat16 h = __float2bfloat16(f); return __builtin_bit_cast(u16, h); }

template <int MODE> struct Cfg;
template <> struct Cfg<1> { static constexpr int BM = 128, BN = 128, NS = 4, NK = 16, NKSEG = 16, LDP = D_, LDQ = D_;
                            static constexpr int TM = S_ / 128, TN = 2 * D_ / 128, XM = 4; static constexpr bool PTR = false; };
template <> struct Cfg<2> { static constexpr int BM = 128, BN = 64, NS = 4, NK = 64, NKSEG = 32, LDP = S_, LDQ = S_;
                            static constexpr int TM = D_ / 128, TN = S_ / 64, XM = 4; static constexpr bool PTR = false; };
template <> struct Cfg<3> { static constexpr int BM = 256, BN = 256, NS = 2, NK = 16, NKSEG = 16, LDP = F_, LDQ = D_;
                            static constexpr int TM = F_ / 128, TN = S_ / 256, XM = 7; static constexpr bool PTR = true; };
template <> struct Cfg<4> { static constexpr int BM = 128, BN = 64, NS = 4, NK = 56, NKSEG = 56, LDP = D_, LDQ = F_;
                            static constexpr int TM = D_ / 128, TN = S_ / 64, XM = 4; static constexpr bool PTR = true; };

template <int MODE> constexpr int lds_bytes() {
  return Cfg<MODE>::NS * (Cfg<MODE>::BM + Cfg<MODE>::BN) * 128 + 4096;
}

__device__ __forceinline__ void glds16(const u16* g, char* l) {
  __builtin_amdgcn_global_load_lds((const unsigned*)g, (unsigned*)l, 16, 0, 0);
}
__device__ __forceinline__ bf16x4 tr_read(const char* p) {
  return __builtin_amdgcn_ds_read_tr16_b64_v4i16((LDS_AS bf16x4*)(p));
}

__device__ __forceinline__ float2 merge_row(const float2* __restrict__ part) {
  float m[NPART], q = 0.f, ms = 0.f;
#pragma unroll
  for (int i = 0; i < NPART; ++i) { float2 v = part[i]; m[i] = v.x; q += v.y; ms += v.x; }
  const float mean = ms * (1.0f / NPART);
  float dd = 0.f;
#pragma unroll
  for (int i = 0; i < NPART; ++i) { float d = m[i] - mean; dd += d * d; }
  const float var = (q + 64.0f * dd) * (1.0f / D_);
  return make_float2(mean, 1.0f / sqrtf(var + LN_EPS));
}

template <int MODE>
__global__ __launch_bounds__(256, 1) void gemm_k(const u16* __restrict__ p0, const u16* __restrict__ p1,
                                                 const u16* __restrict__ q0, const u16* __restrict__ q1,
                                                 const u16* __restrict__ xin, u16* __restrict__ out,
                                                 const float2* __restrict__ stats_in, float2* __restrict__ stats_out,
                                                 const float* __restrict__ cs) {
  using C = Cfg<MODE>;
  constexpr int BM = C::BM, BN = C::BN, NS = C::NS, NK = C::NK;
  constexpr bool PTR = C::PTR;
  constexpr int WM = BM / 2, WN = BN / 2, MF = WM / 16, NF = WN / 16;
  constexpr int PBYTES = BM * 128, QBYTES = BN * 128, STAGE = PBYTES + QBYTES;
  constexpr int PI = BM / 32, QI = BN / 32, PER = PI + QI;
  extern __shared__ __attribute__((aligned(16))) char lds[];
  float2* st_lds = (float2*)(lds + NS * STAGE);
  float* cs_lds = (float*)(lds + NS * STAGE + 2048);

  const int tid = threadIdx.x, lane = tid & 63, w = tid >> 6;
  const int wm = w >> 1, wn = w & 1;
  const int fr = lane & 15, fq = lane >> 4;

  int tm, tn;
  {
    const int b = blockIdx.x, xcd = b & 7, idx = b >> 3;
    constexpr int XM = C::XM, NXM = C::TM / XM;
    const int xm = xcd % NXM, xn = xcd / NXM;
    tm = xm * XM + idx % XM;
    tn = xn * (C::TN / (8 / NXM)) + idx / XM;
  }
  const int m0 = tm * (MODE == 3 ? 128 : BM), n0 = tn * BN;

  const u16 *pb0, *pb1, *qb0, *qb1;
  if constexpr (MODE == 1) {
    pb0 = pb1 = p0 + (size_t)m0 * C::LDP;
    qb0 = qb1 = (n0 < D_ ? q0 + (size_t)n0 * C::LDQ : q1 + (size_t)(n0 - D_) * C::LDQ);
  } else if constexpr (MODE == 2) {
    pb0 = p0 + (size_t)m0 * C::LDP; pb1 = p0 + (size_t)(D_ + m0) * C::LDP;
    qb0 = q0 + (size_t)n0 * C::LDQ; qb1 = q1 + (size_t)n0 * C::LDQ;
  } else if constexpr (MODE == 3) {
    pb0 = p0 + m0; pb1 = p1 + m0;
    qb0 = qb1 = q0 + (size_t)n0 * C::LDQ;
  } else {
    pb0 = pb1 = p0 + m0;
    qb0 = qb1 = q0 + (size_t)n0 * C::LDQ;
  }

  const int kc_off_p = (lane >> 3) * C::LDP + (((lane & 7) ^ (lane >> 3)) << 3);
  const int kc_off_q = (lane >> 3) * C::LDQ + (((lane & 7) ^ (lane >> 3)) << 3);
  const int tr_col0 = ((((lane & 15) >> 1) ^ ((lane >> 4) & 3)) << 4) + ((lane & 1) << 3);
  const int tr_col1 = ((((lane & 15) >> 1) ^ (((lane >> 4) & 3) | 4)) << 4) + ((lane & 1) << 3);

  auto issue = [&](int kt, int buf) {
    char* sb = lds + buf * STAGE;
    const bool seg = (MODE == 2) && (kt >= C::NKSEG);
    const int kk = (seg ? kt - C::NKSEG : kt) * 64;
    if constexpr (!PTR) {
      const u16* pp = (seg ? pb1 : pb0) + kk;
#pragma unroll
      for (int i = 0; i < PI; ++i) {
        const int ins = w * PI + i;
        glds16(pp + (size_t)(ins * 8) * C::LDP + kc_off_p, sb + ins * 1024 + lane * 16);
      }
    } else {
#pragma unroll
      for (int i = 0; i < PI; ++i) {
        const int ins = w * PI + i;
        const int sub = ins >> 4, ii = ins & 15;
        const u16* pp = (sub ? pb1 : pb0);
        const int col = ((ii >> 1) & 1) ? tr_col1 : tr_col0;
        glds16(pp + (size_t)(kk + 4 * ii + (lane >> 4)) * C::LDP + col, sb + ins * 1024 + lane * 16);
      }
    }
    const u16* qq = (seg ? qb1 : qb0) + kk;
#pragma unroll
    for (int i = 0; i < QI; ++i) {
      const int ins = w * QI + i;
      glds16(qq + (size_t)(ins * 8) * C::LDQ + kc_off_q, sb + PBYTES + ins * 1024 + lane * 16);
    }
  };

  if constexpr (MODE == 1) {
    if (tid < 128) st_lds[tid] = merge_row(stats_in + (size_t)(m0 + tid) * NPART);
  } else if constexpr (MODE == 3) {
    st_lds[tid] = merge_row(stats_in + (size_t)(n0 + tid) * NPART);
    const int sub = tid >> 7, c = m0 + (tid & 127);
    float s = 0.f;
#pragma unroll
    for (int r = 0; r < CSG; ++r) s += cs[r * (2 * F_) + sub * F_ + c];
    cs_lds[tid] = s;
  }

  const int tr_g = lane >> 4, tr_q = (lane & 15) >> 2, tr_p = lane & 3;
  const int tr_f = tr_q | ((tr_g & 1) << 2);
  const int tr_base = (8 * tr_g + tr_q) * 256 + tr_p * 8;
  auto read_set = [&](int buf, int kh, bf16x8(&pf)[MF], bf16x8(&qf)[NF]) {
    const char* sb = lds + buf * STAGE;
    if constexpr (!PTR) {
#pragma unroll
      for (int mi = 0; mi < MF; ++mi)
        pf[mi] = *(const bf16x8*)(sb + (wm * WM + mi * 16 + fr) * 128 + (((kh * 4 + fq) ^ (fr & 7)) << 4));
    } else {
#pragma unroll
      for (int mi = 0; mi < MF; ++mi) {
        int sub, mf;
        if constexpr (MODE == 3) { sub = mi / (MF / 2); mf = wm * (MF / 2) + (mi % (MF / 2)); }
        else { sub = 0; mf = wm * MF + mi; }
        const char* a = sb + sub * 16384 + kh * (32 * 256) + tr_base + ((mf ^ tr_f) << 5);
        bf16x4 lo = tr_read(a), hi = tr_read(a + 4 * 256);
        pf[mi] = __builtin_shufflevector(lo, hi, 0, 1, 2, 3, 4, 5, 6, 7);
      }
    }
#pragma unroll
    for (int ni = 0; ni < NF; ++ni)
      qf[ni] = *(const bf16x8*)(sb + PBYTES + (wn * WN + ni * 16 + fr) * 128 + (((kh * 4 + fq) ^ (fr & 7)) << 4));
  };

  f32x4 acc[MF][NF];
#pragma unroll
  for (int mi = 0; mi < MF; ++mi)
#pragma unroll
    for (int ni = 0; ni < NF; ++ni) acc[mi][ni] = (f32x4){0.f, 0.f, 0.f, 0.f};

  auto mma = [&](const bf16x8(&pf)[MF], const bf16x8(&qf)[NF]) {
#pragma unroll
    for (int mi = 0; mi < MF; ++mi)
#pragma unroll
      for (int ni = 0; ni < NF; ++ni)
        acc[mi][ni] = __builtin_amdgcn_mfma_f32_16x16x32_bf16(pf[mi], qf[ni], acc[mi][ni], 0, 0, 0);
  };

  bf16x8 pA[MF], qA[NF], pB[MF], qB[NF];
#pragma unroll
  for (int s = 0; s < NS; ++s) issue(s, s);
  if constexpr (NS == 4) asm volatile("s_waitcnt vmcnt(%0)" ::"n"(3 * PER) : "memory");
  else asm volatile("s_waitcnt vmcnt(%0)" ::"n"(PER) : "memory");
  __builtin_amdgcn_s_barrier();
  read_set(0, 0, pA, qA);
  for (int kt = 0; kt < NK; ++kt) {
    const int buf = kt & (NS - 1);
    read_set(buf, 1, pB, qB);
    mma(pA, qA);
    if (kt + 1 < NK) {
      if constexpr (NS == 4) asm volatile("s_waitcnt vmcnt(%0) lgkmcnt(0)" ::"n"(2 * PER) : "memory");
      else asm volatile("s_waitcnt vmcnt(0) lgkmcnt(0)" ::: "memory");
      __builtin_amdgcn_s_barrier();
      const int kn = (kt + NS < NK) ? kt + NS : NK - 1;
      issue(kn, buf);
      read_set((kt + 1) & (NS - 1), 0, pA, qA);
    }
    mma(pB, qB);
  }
  asm volatile("s_waitcnt vmcnt(0)" ::: "memory");

  if constexpr (MODE == 1) {
#pragma unroll
    for (int mi = 0; mi < MF; ++mi) {
      const int sl = wm * WM + mi * 16 + fq * 4;
      const f32x4 st01 = *(const f32x4*)(&st_lds[sl]), st23 = *(const f32x4*)(&st_lds[sl + 2]);
#pragma unroll
      for (int ni = 0; ni < NF; ++ni) {
        const int e = n0 + wn * WN + ni * 16 + fr;
        const float c = cs[e];
        const float sg = (e >= D_) ? -1.f : 1.f;
        u16x4 o;
        o[0] = f2bf(sg * st01[1] * (acc[mi][ni][0] - st01[0] * c));
        o[1] = f2bf(sg * st01[3] * (acc[mi][ni][1] - st01[2] * c));
        o[2] = f2bf(sg * st23[1] * (acc[mi][ni][2] - st23[0] * c));
        o[3] = f2bf(sg * st23[3] * (acc[mi][ni][3] - st23[2] * c));
        *(u16x4*)(out + (size_t)e * S_ + m0 + sl) = o;
      }
    }
  } else if constexpr (MODE == 3) {
    constexpr int HF = MF / 2;
#pragma unroll
    for (int ni = 0; ni < NF; ++ni) {
      const int rl = wn * WN + ni * 16 + fr;
      const float2 st = st_lds[rl];
#pragma unroll
      for (int mi = 0; mi < HF; ++mi) {
        const int cl = wm * (WM / 2) + mi * 16 + fq * 4;
        const f32x4 cg = *(const f32x4*)(&cs_lds[cl]), cu = *(const f32x4*)(&cs_lds[128 + cl]);
        u16x4 o;
#pragma unroll
        for (int j = 0; j < 4; ++j) {
          const float g = st.y * (acc[mi][ni][j] - st.x * cg[j]);
          const float u = st.y * (acc[HF + mi][ni][j] - st.x * cu[j]);
          o[j] = f2bf(u / (1.0f + __expf(-g)));
        }
        *(u16x4*)(out + (size_t)(n0 + rl) * F_ + m0 + cl) = o;
      }
    }
  } else {
#pragma unroll
    for (int ni = 0; ni < NF; ++ni) {
      const int row = n0 + wn * WN + ni * 16 + fr;
      float v[MF][4];
      float s = 0.f;
#pragma unroll
      for (int mi = 0; mi < MF; ++mi) {
        const int col = m0 + wm * WM + mi * 16 + fq * 4;
        const u16x4 xi = *(const u16x4*)(xin + (size_t)row * D_ + col);
        u16x4 o;
#pragma unroll
        for (int j = 0; j < 4; ++j) {
          o[j] = f2bf(bf2f(xi[j]) + acc[mi][ni][j]);
          v[mi][j] = bf2f(o[j]);
          s += v[mi][j];
        }
        *(u16x4*)(out + (size_t)row * D_ + col) = o;
      }
      if (stats_out != nullptr) {
        s += __shfl_xor(s, 16); s += __shfl_xor(s, 32);
        const float mw = s * (1.0f / 64.0f);
        float q = 0.f;
#pragma unroll
        for (int mi = 0; mi < MF; ++mi)
#pragma unroll
          for (int j = 0; j < 4; ++j) { const float d = v[mi][j] - mw; q += d * d; }
        q += __shfl_xor(q, 16); q += __shfl_xor(q, 32);
        if (fq == 0) stats_out[(size_t)row * NPART + ((m0 + wm * WM) >> 6)] = make_float2(mw, q);
      }
    }
  }
}

__global__ __launch_bounds__(256) void prep_k(const u16* __restrict__ x, const u16* __restrict__ wre,
                                              const u16* __restrict__ wim, const u16* __restrict__ wg,
                                              const u16* __restrict__ wu, float2* __restrict__ stats,
                                              float* __restrict__ cs1, float* __restrict__ csp) {
  const int b = blockIdx.x, tid = threadIdx.x, lane = tid & 63, w = tid >> 6;
  if (b < 512) {
    const int row = b * 4 + w;
    const u16* p = x + (size_t)row * D_ + lane * 16;
    bf16x8 a = *(const bf16x8*)p, c = *(const bf16x8*)(p + 8);
    float v[16], s = 0.f;
#pragma unroll
    for (int j = 0; j < 8; ++j) { v[j] = bf2f((u16)a[j]); v[8 + j] = bf2f((u16)c[j]); }
#pragma unroll
    for (int j = 0; j < 16; ++j) s += v[j];
    s += __shfl_xor(s, 1); s += __shfl_xor(s, 2);
    const float mw = s * (1.0f / 64.0f);
    float q = 0.f;
#pragma unroll
    for (int j = 0; j < 16; ++j) { const float d = v[j] - mw; q += d * d; }
    q += __shfl_xor(q, 1); q += __shfl_xor(q, 2);
    if ((lane & 3) == 0) stats[(size_t)row * NPART + (lane >> 2)] = make_float2(mw, q);
  } else if (b < 1024) {
    const int row = (b - 512) * 4 + w;
    const u16* p = (row < D_ ? wre + (size_t)row * D_ : wim + (size_t)(row - D_) * D_) + lane * 16;
    bf16x8 a = *(const bf16x8*)p, c = *(const bf16x8*)(p + 8);
    float s = 0.f;
#pragma unroll
    for (int j = 0; j < 8; ++j) s += bf2f((u16)a[j]) + bf2f((u16)c[j]);
#pragma unroll
    for (int o = 1; o < 64; o <<= 1) s += __shfl_xor(s, o);
    if (lane == 0) cs1[row] = s;
  } else {
    const int id = b - 1024;
    const int rg = id / 14, cb = id % 14;
    const int sub = cb / 7, c = (cb % 7) * 512 + tid * 2;
    const u16* p = (sub ? wu : wg) + (size_t)(rg * 64) * F_ + c;
    float s0 = 0.f, s1 = 0.f;
#pragma unroll 16
    for (int r = 0; r < 64; ++r) {
      const unsigned v = *(const unsigned*)(p + (size_t)r * F_);
      s0 += __uint_as_float(v << 16); s1 += __uint_as_float(v & 0xffff0000u);
    }
    csp[rg * (2 * F_) + sub * F_ + c] = s0;
    csp[rg * (2 * F_) + sub * F_ + c + 1] = s1;
  }
}

extern "C" void kernel_launch(void* const* d_in, const int* in_sizes, int n_in, void* d_out, int out_size,
                              void* d_ws, size_t ws_size, hipStream_t stream) {
  const u16* x0 = (const u16*)d_in[0];
  const u16* cr = (const u16*)d_in[1];
  const u16* ci = (const u16*)d_in[2];
  const u16* wre = (const u16*)d_in[3];
  const u16* wim = (const u16*)d_in[4];
  const u16* wg = (const u16*)d_in[6];
  const u16* wu = (const u16*)d_in[7];
  const u16* wd = (const u16*)d_in[8];
  u16* outp = (u16*)d_out;

  char* ws = (char*)d_ws;
  u16* abT = (u16*)(ws);
  u16* xa = (u16*)(ws + (8u << 20));
  u16* xb = (u16*)(ws + (12u << 20));
  u16* h = (u16*)(ws + (16u << 20));
  float2* stats = (float2*)(ws + (32u << 20));
  float* cs1 = (float*)(ws + (32u << 20) + (512u << 10));
  float* csp = (float*)(ws + (33u << 20));

  {
    hipFuncSetAttribute((const void*)gemm_k<1>, hipFuncAttributeMaxDynamicSharedMemorySize, lds_bytes<1>());
    hipFuncSetAttribute((const void*)gemm_k<2>, hipFuncAttributeMaxDynamicSharedMemorySize, lds_bytes<2>());
    hipFuncSetAttribute((const void*)gemm_k<3>, hipFuncAttributeMaxDynamicSharedMemorySize, lds_bytes<3>());
    hipFuncSetAttribute((const void*)gemm_k<4>, hipFuncAttributeMaxDynamicSharedMemorySize, lds_bytes<4>());
  }

  prep_k<<<1024 + CSG * 14, 256, 0, stream>>>(x0, wre, wim, wg, wu, stats, cs1, csp);
  const u16* xcur = x0;
  u16* xbuf[2] = {xa, xb};
  for (int layer = 0; layer < 2; ++layer) {
    u16* x1 = xbuf[0];
    u16* x2 = (layer == 0) ? xbuf[1] : outp;
    gemm_k<1><<<Cfg<1>::TM * Cfg<1>::TN, 256, lds_bytes<1>(), stream>>>(xcur, nullptr, wre, wim, nullptr, abT, stats, nullptr, cs1);
    gemm_k<2><<<Cfg<2>::TM * Cfg<2>::TN, 256, lds_bytes<2>(), stream>>>(abT, nullptr, cr, ci, xcur, x1, nullptr, stats, nullptr);
    gemm_k<3><<<Cfg<3>::TM * Cfg<3>::TN, 256, lds_bytes<3>(), stream>>>(wg, wu, x1, nullptr, nullptr, h, stats, nullptr, csp);
    gemm_k<4><<<Cfg<4>::TM * Cfg<4>::TN, 256, lds_bytes<4>(), stream>>>(wd, nullptr, h, nullptr, x1, x2, nullptr, layer == 0 ? stats : nullptr, nullptr);
    xcur = x2;
  }
}
```

```cpp
#include <hip/hip_runtime.h>
#include <hip/hip_bf16.h>
#include <stdint.h>
#include <type_traits>

typedef unsigned short u16;
typedef __attribute__((ext_vector_type(8))) short bf16x8;
typedef __attribute__((ext_vector_type(4))) short bf16x4;
typedef __attribute__((ext_vector_type(4))) float f32x4;
typedef __attribute__((ext_vector_type(4))) unsigned short u16x4;
#define LDS_AS __attribute__((address_space(3)))

constexpr int S_ = 2048, D_ = 1024, F_ = 3584;
constexpr int NPART = 16;
constexpr int CSG = 16;
constexpr float LN_EPS = 1e-5f;

__device__ __forceinline__ float bf2f(u16 h) { return __uint_as_float(((unsigned)h) << 16); }
__device__ __forceinline__ u16 f2bf(float f) { __hip_bfloat16 h = __float2bfloat16(f); return __builtin_bit_cast(u16, h); }

template <int MODE> struct Cfg;
template <> struct Cfg<1> { static constexpr int BM = 128, BN = 128, NS = 4, NK = 16, NKSEG = 16, LDP = D_, LDQ = D_;
                            static constexpr int TM = S_ / 128, TN = 2 * D_ / 128, XM = 4; static constexpr bool PTR = false; };
template <> struct Cfg<2> { static constexpr int BM = 128, BN = 64, NS = 4, NK = 64, NKSEG = 32, LDP = S_, LDQ = S_;
                            static constexpr int TM = D_ / 128, TN = S_ / 64, XM = 4; static constexpr bool PTR = false; };
template <> struct Cfg<3> { static constexpr int BM = 256, BN = 256, NS = 2, NK = 16, NKSEG = 16, LDP = F_, LDQ = D_;
                            static constexpr int TM = F_ / 128, TN = S_ / 256, XM = 7; static constexpr bool PTR = true; };
template <> struct Cfg<4> { static constexpr int BM = 128, BN = 64, NS = 4, NK = 56, NKSEG = 56, LDP = D_, LDQ = F_;
                            static constexpr int TM = D_ / 128, TN = S_ / 64, XM = 4; static constexpr bool PTR = true; };

template <int MODE> constexpr int lds_bytes() {
  return Cfg<MODE>::NS * (Cfg<MODE>::BM + Cfg<MODE>::BN) * 128 + 4096;
}

__device__ __forceinline__ void glds16(unsigned voff, const void* sbase, unsigned lds_addr) {
  asm volatile("s_mov_b32 m0, %2\n\ts_nop 0\n\tglobal_load_lds_dwordx4 %0, %1" ::"v"(voff), "s"(sbase), "s"(lds_addr) : "memory");
}
template <int OFF>
__device__ __forceinline__ bf16x4 tr_read(unsigned addr) {
  return __builtin_amdgcn_ds_read_tr16_b64_v4i16((LDS_AS bf16x4*)(size_t)(addr + OFF));
}

__device__ __forceinline__ float2 merge_row(const float2* __restrict__ part) {
  float m[NPART], q = 0.f, ms = 0.f;
#pragma unroll
  for (int i = 0; i < NPART; ++i) { float2 v = part[i]; m[i] = v.x; q += v.y; ms += v.x; }
  const float mean = ms * (1.0f / NPART);
  float dd = 0.f;
#pragma unroll
  for (int i = 0; i < NPART; ++i) { float d = m[i] - mean; dd += d * d; }
  const float var = (q + 64.0f * dd) * (1.0f / D_);
  return make_float2(mean, 1.0f / sqrtf(var + LN_EPS));
}

template <int MODE>
__global__ __launch_bounds__(512, 2) void gemm_k(const u16* __restrict__ p0, const u16* __restrict__ p1,
                                                 const u16* __restrict__ q0, const u16* __restrict__ q1,
                                                 const u16* __restrict__ xin, u16* __restrict__ out,
                                                 const float2* __restrict__ stats_in, float2* __restrict__ stats_out,
                                                 const float* __restrict__ cs) {
  using C = Cfg<MODE>;
  constexpr int BM = C::BM, BN = C::BN, NS = C::NS, NK = C::NK;
  constexpr bool PTR = C::PTR;
  constexpr bool KSPLIT = (MODE != 3);
  constexpr int NWN = KSPLIT ? 2 : 4;
  constexpr int WM = BM / 2, WN = BN / NWN, MF = WM / 16, NF = WN / 16;
  constexpr int PBYTES = BM * 128, QBYTES = BN * 128, STAGE = PBYTES + QBYTES;
  constexpr int PI = BM / 64, QI = BN / 64, PER = PI + QI;
  extern __shared__ __attribute__((aligned(16))) char lds[];
  float2* st_lds = (float2*)(lds + NS * STAGE);
  float* cs_lds = (float*)(lds + NS * STAGE + 2048);

  const int tid = threadIdx.x, lane = tid & 63;
  const int w = __builtin_amdgcn_readfirstlane(tid >> 6);
  const int kg = KSPLIT ? (w >> 2) : 0;
  const int wm = KSPLIT ? ((w >> 1) & 1) : (w >> 2), wn = KSPLIT ? (w & 1) : (w & 3);
  const int fr = lane & 15, fq = lane >> 4;
  const unsigned lds0 = (unsigned)(size_t)(LDS_AS char*)lds;

  int tm, tn;
  {
    const int b = blockIdx.x, xcd = b & 7, idx = b >> 3;
    constexpr int XM = C::XM, NXM = C::TM / XM;
    const int xm = xcd % NXM, xn = xcd / NXM;
    tm = xm * XM + idx % XM;
    tn = xn * (C::TN / (8 / NXM)) + idx / XM;
  }
  const int m0 = tm * (MODE == 3 ? 128 : BM), n0 = tn * BN;

  const u16 *pb0, *pb1, *qb0, *qb1;
  if constexpr (MODE == 1) {
    pb0 = pb1 = p0 + (size_t)m0 * C::LDP;
    qb0 = qb1 = (n0 < D_ ? q0 + (size_t)n0 * C::LDQ : q1 + (size_t)(n0 - D_) * C::LDQ);
  } else if constexpr (MODE == 2) {
    pb0 = p0 + (size_t)m0 * C::LDP; pb1 = p0 + (size_t)(D_ + m0) * C::LDP;
    qb0 = q0 + (size_t)n0 * C::LDQ; qb1 = q1 + (size_t)n0 * C::LDQ;
  } else if constexpr (MODE == 3) {
    pb0 = pb1 = ((w >> 2) ? p1 : p0) + m0;
    qb0 = qb1 = q0 + (size_t)n0 * C::LDQ;
  } else {
    pb0 = pb1 = p0 + m0;
    qb0 = qb1 = q0 + (size_t)n0 * C::LDQ;
  }

  unsigned voff_p[PI], voff_q[QI];
#pragma unroll
  for (int i = 0; i < PI; ++i) {
    if constexpr (!PTR) {
      voff_p[i] = (unsigned)(((w * PI + i) * 8 + (lane >> 3)) * C::LDP * 2 + (((lane & 7) ^ (lane >> 3)) << 4));
    } else {
      const int ii = (w * PI + i) & 15;
      const int f = ((lane >> 4) & 3) | (((ii >> 1) & 1) << 2);
      const int col = ((((lane & 15) >> 1) ^ f) << 4) + ((lane & 1) << 3);
      voff_p[i] = (unsigned)(((4 * ii + (lane >> 4)) * C::LDP + col) * 2);
    }
  }
#pragma unroll
  for (int i = 0; i < QI; ++i)
    voff_q[i] = (unsigned)(((w * QI + i) * 8 + (lane >> 3)) * C::LDQ * 2 + (((lane & 7) ^ (lane >> 3)) << 4));
  const unsigned lds_wp = lds0 + w * PI * 1024, lds_wq = lds0 + PBYTES + w * QI * 1024;

  auto issue = [&](int kt, int buf) {
    const bool seg = (MODE == 2) && (kt >= C::NKSEG);
    const int kk = (seg ? kt - C::NKSEG : kt) * 64;
    const u16* pp = (seg ? pb1 : pb0) + (PTR ? (size_t)kk * C::LDP : (size_t)kk);
    const u16* qq = (seg ? qb1 : qb0) + kk;
#pragma unroll
    for (int i = 0; i < PI; ++i) glds16(voff_p[i], pp, lds_wp + buf * STAGE + i * 1024);
#pragma unroll
    for (int i = 0; i < QI; ++i) glds16(voff_q[i], qq, lds_wq + buf * STAGE + i * 1024);
  };

  if (w < 4) __builtin_amdgcn_s_setprio(1);
  auto issue_piece = [&](int kt, int buf, int i) {
    const bool seg = (MODE == 2) && (kt >= C::NKSEG);
    const int kk = (seg ? kt - C::NKSEG : kt) * 64;
    if (i < PI) glds16(voff_p[i < PI ? i : 0], (seg ? pb1 : pb0) + (PTR ? (size_t)kk * C::LDP : (size_t)kk), lds_wp + buf * STAGE + i * 1024);
    else glds16(voff_q[i >= PI ? i - PI : 0], (seg ? qb1 : qb0) + kk, lds_wq + buf * STAGE + (i - PI) * 1024);
  };
#pragma unroll
  for (int s = 0; s < NS; ++s) issue(s, s);

  if constexpr (MODE == 1) {
    if (tid < 128) st_lds[tid] = merge_row(stats_in + (size_t)(m0 + tid) * NPART);
  } else if constexpr (MODE == 3) {
    if (tid < 256) st_lds[tid] = merge_row(stats_in + (size_t)(n0 + tid) * NPART);
    else {
      const int t2 = tid - 256, sub = t2 >> 7, c = m0 + (t2 & 127);
      float s = 0.f;
#pragma unroll
      for (int r = 0; r < CSG; ++r) s += cs[r * (2 * F_) + sub * F_ + c];
      cs_lds[t2] = s;
    }
  }

  constexpr int NEP = KSPLIT ? NF / 2 : 1;
  u16x4 xres[NEP][MF];
  if constexpr (MODE == 2 || MODE == 4) {
#pragma unroll
    for (int nj = 0; nj < NEP; ++nj) {
      const int row = n0 + wn * WN + (kg * (NF / 2) + nj) * 16 + fr;
#pragma unroll
      for (int mi = 0; mi < MF; ++mi)
        xres[nj][mi] = *(const u16x4*)(xin + (size_t)row * D_ + m0 + wm * WM + mi * 16 + fq * 4);
    }
  }

  unsigned tra[MF];
  if constexpr (PTR) {
    const int g = lane >> 4, q = (lane & 15) >> 2, pp = lane & 3;
    const int f = q | ((g & 1) << 2);
#pragma unroll
    for (int mi = 0; mi < MF; ++mi) {
      int sub, mf;
      if constexpr (MODE == 3) { sub = mi / (MF / 2); mf = wm * (MF / 2) + (mi % (MF / 2)); }
      else { sub = 0; mf = wm * MF + mi; }
      tra[mi] = lds0 + sub * 16384 + (8 * g + q) * 256 + pp * 8 + ((mf ^ f) << 5) + (KSPLIT ? kg * 8192 : 0);
    }
  }
  const int poff = (wm * WM + fr) * 128, qoff = PBYTES + (wn * WN + fr) * 128;
#define READ_SET(BUF, KH, PF, QF)                                                                           \
  do {                                                                                                      \
    const char* sb_ = lds + (BUF) * STAGE;                                                                  \
    const int sw_ = ((((KH) * 4 + fq) ^ (fr & 7)) << 4);                                                    \
    if constexpr (!PTR) {                                                                                   \
      _Pragma("unroll") for (int mi = 0; mi < MF; ++mi)                                                     \
          PF[mi] = *(const bf16x8*)(sb_ + poff + mi * 2048 + sw_);                                          \
    } else {                                                                                                \
      _Pragma("unroll") for (int mi = 0; mi < MF; ++mi) {                                                   \
        const unsigned a_ = tra[mi] + (BUF) * STAGE;                                                        \
        bf16x4 lo_ = tr_read<(KSPLIT ? 0 : (KH)) * 8192>(a_), hi_ = tr_read<(KSPLIT ? 0 : (KH)) * 8192 + 1024>(a_); \
        PF[mi] = __builtin_shufflevector(lo_, hi_, 0, 1, 2, 3, 4, 5, 6, 7);                                 \
      }                                                                                                     \
    }                                                                                                       \
    _Pragma("unroll") for (int ni = 0; ni < NF; ++ni)                                                       \
        QF[ni] = *(const bf16x8*)(sb_ + qoff + ni * 2048 + sw_);                                            \
  } while (0)

  f32x4 acc[MF][NF];
#pragma unroll
  for (int mi = 0; mi < MF; ++mi)
#pragma unroll
    for (int ni = 0; ni < NF; ++ni) acc[mi][ni] = (f32x4){0.f, 0.f, 0.f, 0.f};

#define MFMA_STMT(PF, QF) acc[mi][ni] = __builtin_amdgcn_mfma_f32_16x16x32_bf16(PF[mi], QF[ni], acc[mi][ni], 0, 0, 0)
#define MMA(PF, QF)                                                                                       \
  do {                                                                                                    \
    _Pragma("unroll") for (int mi = 0; mi < MF; ++mi) _Pragma("unroll") for (int ni = 0; ni < NF; ++ni)   \
        MFMA_STMT(PF, QF);                                                                                \
  } while (0)

  if constexpr (KSPLIT) {
    bf16x8 pA[MF], qA[NF], pB[MF], qB[NF];
    asm volatile("s_waitcnt vmcnt(%0)" ::"n"((NS - 1) * PER) : "memory");
    __builtin_amdgcn_s_barrier();
    READ_SET(0, kg, pA, qA);
    for (int kt0 = 0; kt0 < NK; kt0 += NS) {
#pragma unroll
      for (int s = 0; s < NS; ++s) {
        asm volatile("s_waitcnt vmcnt(%0) lgkmcnt(0)" ::"n"((NS - 2) * PER) : "memory");
        __builtin_amdgcn_s_barrier();
        const int kn = (kt0 + s + NS < NK) ? kt0 + s + NS : NK - 1;
        if (s & 1) READ_SET((s + 1) % NS, kg, pA, qA);
        else       READ_SET((s + 1) % NS, kg, pB, qB);
#pragma unroll
        for (int mi = 0; mi < MF; ++mi) {
#pragma unroll
          for (int ni = 0; ni < NF; ++ni) { if (s & 1) MFMA_STMT(pB, qB); else MFMA_STMT(pA, qA); }
          if (mi < PER) issue_piece(kn, s, mi);
        }
      }
    }
  } else {
    bf16x8 pA[MF], qA[NF];
    for (int kt0 = 0; kt0 < NK; kt0 += NS) {
#pragma unroll
      for (int s = 0; s < NS; ++s) {
        asm volatile("s_waitcnt vmcnt(%0) lgkmcnt(0)" ::"n"((NS - 2) * PER) : "memory");
        __builtin_amdgcn_s_barrier();
        const int kn = (kt0 + s + NS - 1 < NK) ? kt0 + s + NS - 1 : NK - 1;
#pragma unroll
        for (int kh = 0; kh < 2; ++kh) {
          const char* sb_ = lds + s * STAGE;
          const int sw_ = (((kh * 4 + fq) ^ (fr & 7)) << 4);
#pragma unroll
          for (int ni = 0; ni < NF; ++ni) qA[ni] = *(const bf16x8*)(sb_ + qoff + ni * 2048 + sw_);
#pragma unroll
          for (int mi = 0; mi < MF; ++mi) {
            const unsigned a_ = tra[mi] + s * STAGE;
            bf16x4 lo_, hi_;
            if (kh == 0) { lo_ = tr_read<0>(a_); hi_ = tr_read<1024>(a_); }
            else { lo_ = tr_read<8192>(a_); hi_ = tr_read<8192 + 1024>(a_); }
            pA[mi] = __builtin_shufflevector(lo_, hi_, 0, 1, 2, 3, 4, 5, 6, 7);
          }
#pragma unroll
          for (int mi = 0; mi < MF; ++mi) {
#pragma unroll
            for (int ni = 0; ni < NF; ++ni) MFMA_STMT(pA, qA);
            if (kh == 0 && kt0 + s > 0) issue_piece(kn, (s + NS - 1) % NS, mi);
            if (mi & 1) __builtin_amdgcn_sched_barrier(0);
          }
          __builtin_amdgcn_sched_barrier(0);
        }
      }
    }
  }
  asm volatile("s_waitcnt vmcnt(0)" ::: "memory");

  auto finish = [&](auto GC) {
    constexpr int G = decltype(GC)::value;
    int tz = threadIdx.x;
    asm volatile("" : "+v"(tz));
    const int lane = tz & 63, fr = lane & 15, fq = lane >> 4;
    constexpr int NE = KSPLIT ? NF / 2 : NF;
    constexpr int nb = KSPLIT ? G * (NF / 2) : 0;
    if constexpr (KSPLIT) {
      __builtin_amdgcn_s_barrier();
      f32x4* xch = (f32x4*)lds;
      constexpr int ob = (1 - G) * (NF / 2);
#pragma unroll
      for (int mi = 0; mi < MF; ++mi)
#pragma unroll
        for (int j = 0; j < NF / 2; ++j) xch[(w * (MF * NF / 2) + mi * (NF / 2) + j) * 64 + lane] = acc[mi][ob + j];
      __syncthreads();
      const int pw = w ^ 4;
#pragma unroll
      for (int mi = 0; mi < MF; ++mi)
#pragma unroll
        for (int j = 0; j < NF / 2; ++j) acc[mi][nb + j] += xch[(pw * (MF * NF / 2) + mi * (NF / 2) + j) * 64 + lane];
    }

    if constexpr (MODE == 1) {
#pragma unroll
      for (int mi = 0; mi < MF; ++mi) {
        const int sl = wm * WM + mi * 16 + fq * 4;
        const f32x4 st01 = *(const f32x4*)(&st_lds[sl]), st23 = *(const f32x4*)(&st_lds[sl + 2]);
#pragma unroll
        for (int nj = 0; nj < NE; ++nj) {
          const int ni = nb + nj;
          const int e = n0 + wn * WN + ni * 16 + fr;
          const float c = cs[e];
          const float sg = (e >= D_) ? -1.f : 1.f;
          u16x4 o;
          o[0] = f2bf(sg * st01[1] * (acc[mi][ni][0] - st01[0] * c));
          o[1] = f2bf(sg * st01[3] * (acc[mi][ni][1] - st01[2] * c));
          o[2] = f2bf(sg * st23[1] * (acc[mi][ni][2] - st23[0] * c));
          o[3] = f2bf(sg * st23[3] * (acc[mi][ni][3] - st23[2] * c));
          *(u16x4*)(out + (size_t)e * S_ + m0 + sl) = o;
        }
      }
    } else if constexpr (MODE == 3) {
      constexpr int HF = MF / 2;
#pragma unroll
      for (int ni = 0; ni < NF; ++ni) {
        const int rl = wn * WN + ni * 16 + fr;
        const float2 st = st_lds[rl];
#pragma unroll
        for (int mi = 0; mi < HF; ++mi) {
          const int cl = wm * (WM / 2) + mi * 16 + fq * 4;
          const f32x4 cg = *(const f32x4*)(&cs_lds[cl]), cu = *(const f32x4*)(&cs_lds[128 + cl]);
          u16x4 o;
#pragma unroll
          for (int j = 0; j < 4; ++j) {
            const float g = st.y * (acc[mi][ni][j] - st.x * cg[j]);
            const float u = st.y * (acc[HF + mi][ni][j] - st.x * cu[j]);
            o[j] = f2bf(u * __builtin_amdgcn_rcpf(1.0f + __expf(-g)));
          }
          *(u16x4*)(out + (size_t)(n0 + rl) * F_ + m0 + cl) = o;
        }
      }
    } else {
#pragma unroll
      for (int nj = 0; nj < NE; ++nj) {
        const int ni = nb + nj;
        const int row = n0 + wn * WN + ni * 16 + fr;
        float v[MF][4];
        float s = 0.f;
#pragma unroll
        for (int mi = 0; mi < MF; ++mi) {
          const int col = m0 + wm * WM + mi * 16 + fq * 4;
          const u16x4 xi = xres[nj][mi];
          u16x4 o;
#pragma unroll
          for (int j = 0; j < 4; ++j) {
            o[j] = f2bf(bf2f(xi[j]) + acc[mi][ni][j]);
            v[mi][j] = bf2f(o[j]);
            s += v[mi][j];
          }
          *(u16x4*)(out + (size_t)row * D_ + col) = o;
        }
        if (stats_out != nullptr) {
          s += __shfl_xor(s, 16); s += __shfl_xor(s, 32);
          const float mw = s * (1.0f / 64.0f);
          float q = 0.f;
#pragma unroll
          for (int mi = 0; mi < MF; ++mi)
#pragma unroll
            for (int j = 0; j < 4; ++j) { const float d = v[mi][j] - mw; q += d * d; }
          q += __shfl_xor(q, 16); q += __shfl_xor(q, 32);
          if (fq == 0) stats_out[(size_t)row * NPART + ((m0 + wm * WM) >> 6)] = make_float2(mw, q);
        }
      }
    }
  };
  if (kg == 0) finish(std::integral_constant<int, 0>{});
  else finish(std::integral_constant<int, 1>{});
}

__global__ __launch_bounds__(256) void prep_k(const u16* __restrict__ x, const u16* __restrict__ wre,
                                              const u16* __restrict__ wim, const u16* __restrict__ wg,
                                              const u16* __restrict__ wu, float2* __restrict__ stats,
                                              float* __restrict__ cs1, float* __restrict__ csp) {
  const int b = blockIdx.x, tid = threadIdx.x, lane = tid & 63, w = tid >> 6;
  if (b < 512) {
    const int row = b * 4 + w;
    const u16* p = x + (size_t)row * D_ + lane * 16;
    bf16x8 a = *(const bf16x8*)p, c = *(const bf16x8*)(p + 8);
    float v[16], s = 0.f;
#pragma unroll
    for (int j = 0; j < 8; ++j) { v[j] = bf2f((u16)a[j]); v[8 + j] = bf2f((u16)c[j]); }
#pragma unroll
    for (int j = 0; j < 16; ++j) s += v[j];
    s += __shfl_xor(s, 1); s += __shfl_xor(s, 2);
    const float mw = s * (1.0f / 64.0f);
    float q = 0.f;
#pragma unroll
    for (int j = 0; j < 16; ++j) { const float d = v[j] - mw; q += d * d; }
    q += __shfl_xor(q, 1); q += __shfl_xor(q, 2);
    if ((lane & 3) == 0) stats[(size_t)row * NPART + (lane >> 2)] = make_float2(mw, q);
  } else if (b < 1024) {
    const int row = (b - 512) * 4 + w;
    const u16* p = (row < D_ ? wre + (size_t)row * D_ : wim + (size_t)(row - D_) * D_) + lane * 16;
    bf16x8 a = *(const bf16x8*)p, c = *(const bf16x8*)(p + 8);
    float s = 0.f;
#pragma unroll
    for (int j = 0; j < 8; ++j) s += bf2f((u16)a[j]) + bf2f((u16)c[j]);
#pragma unroll
    for (int o = 1; o < 64; o <<= 1) s += __shfl_xor(s, o);
    if (lane == 0) cs1[row] = s;
  } else {
    const int id = b - 1024;
    const int rg = id / 14, cb = id % 14;
    const int sub = cb / 7, c = (cb % 7) * 512 + tid * 2;
    const u16* p = (sub ? wu : wg) + (size_t)(rg * 64) * F_ + c;
    float s0 = 0.f, s1 = 0.f;
#pragma unroll 16
    for (int r = 0; r < 64; ++r) {
      const unsigned v = *(const unsigned*)(p + (size_t)r * F_);
      s0 += __uint_as_float(v << 16); s1 += __uint_as_float(v & 0xffff0000u);
    }
    csp[rg * (2 * F_) + sub * F_ + c] = s0;
    csp[rg * (2 * F_) + sub * F_ + c + 1] = s1;
  }
}

extern "C" void kernel_launch(void* const* d_in, const int* in_sizes, int n_in, void* d_out, int out_size,
                              void* d_ws, size_t ws_size, hipStream_t stream) {
  const u16* x0 = (const u16*)d_in[0];
  const u16* cr = (const u16*)d_in[1];
  const u16* ci = (const u16*)d_in[2];
  const u16* wre = (const u16*)d_in[3];
  const u16* wim = (const u16*)d_in[4];
  const u16* wg = (const u16*)d_in[6];
  const u16* wu = (const u16*)d_in[7];
  const u16* wd = (const u16*)d_in[8];
  u16* outp = (u16*)d_out;

  char* ws = (char*)d_ws;
  u16* abT = (u16*)(ws);
  u16* xa = (u16*)(ws + (8u << 20));
  u16* xb = (u16*)(ws + (12u << 20));
  u16* h = (u16*)(ws + (16u << 20));
  float2* stats = (float2*)(ws + (32u << 20));
  float* cs1 = (float*)(ws + (32u << 20) + (512u << 10));
  float* csp = (float*)(ws + (33u << 20));

  {
    hipFuncSetAttribute((const void*)gemm_k<1>, hipFuncAttributeMaxDynamicSharedMemorySize, lds_bytes<1>());
    hipFuncSetAttribute((const void*)gemm_k<2>, hipFuncAttributeMaxDynamicSharedMemorySize, lds_bytes<2>());
    hipFuncSetAttribute((const void*)gemm_k<3>, hipFuncAttributeMaxDynamicSharedMemorySize, lds_bytes<3>());
    hipFuncSetAttribute((const void*)gemm_k<4>, hipFuncAttributeMaxDynamicSharedMemorySize, lds_bytes<4>());
  }

  prep_k<<<1024 + CSG * 14, 256, 0, stream>>>(x0, wre, wim, wg, wu, stats, cs1, csp);
  const u16* xcur = x0;
  u16* xbuf[2] = {xa, xb};
  for (int layer = 0; layer < 2; ++layer) {
    u16* x1 = xbuf[0];
    u16* x2 = (layer == 0) ? xbuf[1] : outp;
    gemm_k<1><<<Cfg<1>::TM * Cfg<1>::TN, 512, lds_bytes<1>(), stream>>>(xcur, nullptr, wre, wim, nullptr, abT, stats, nullptr, cs1);
    gemm_k<2><<<Cfg<2>::TM * Cfg<2>::TN, 512, lds_bytes<2>(), stream>>>(abT, nullptr, cr, ci, xcur, x1, nullptr, stats, nullptr);
    gemm_k<3><<<Cfg<3>::TM * Cfg<3>::TN, 512, lds_bytes<3>(), stream>>>(wg, wu, x1, nullptr, nullptr, h, stats, nullptr, csp);
    gemm_k<4><<<Cfg<4>::TM * Cfg<4>::TN, 512, lds_bytes<4>(), stream>>>(wd, nullptr, h, nullptr, x1, x2, nullptr, layer == 0 ? stats : nullptr, nullptr);
    xcur = x2;
  }
}
```

```cpp
#include <hip/hip_runtime.h>
#include <hip/hip_bf16.h>
#include <stdint.h>
#include <type_traits>

typedef unsigned short u16;
typedef __attribute__((ext_vector_type(8))) short bf16x8;
typedef __attribute__((ext_vector_type(4))) short bf16x4;
typedef __attribute__((ext_vector_type(4))) float f32x4;
typedef __attribute__((ext_vector_type(4))) unsigned short u16x4;
#define LDS_AS __attribute__((address_space(3)))

constexpr int S_ = 2048, D_ = 1024, F_ = 3584;
constexpr int NPART = 16;
constexpr int CSG = 16;
constexpr float LN_EPS = 1e-5f;

__device__ __forceinline__ float bf2f(u16 h) { return __uint_as_float(((unsigned)h) << 16); }
__device__ __forceinline__ u16 f2bf(float f) { __hip_bfloat16 h = __float2bfloat16(f); return __builtin_bit_cast(u16, h); }

template <int MODE> struct Cfg;
template <> struct Cfg<1> { static constexpr int BM = 128, BN = 128, NS = 4, NK = 16, NKSEG = 16, LDP = D_, LDQ = D_;
                            static constexpr int TM = S_ / 128, TN = 2 * D_ / 128, XM = 4; static constexpr bool PTR = false; };
template <> struct Cfg<2> { static constexpr int BM = 128, BN = 64, NS = 4, NK = 64, NKSEG = 32, LDP = S_, LDQ = S_;
                            static constexpr int TM = D_ / 128, TN = S_ / 64, XM = 4; static constexpr bool PTR = false; };
template <> struct Cfg<3> { static constexpr int BM = 256, BN = 256, NS = 2, NK = 16, NKSEG = 16, LDP = F_, LDQ = D_;
                            static constexpr int TM = F_ / 128, TN = S_ / 256, XM = 7; static constexpr bool PTR = true; };
template <> struct Cfg<4> { static constexpr int BM = 128, BN = 64, NS = 4, NK = 56, NKSEG = 56, LDP = D_, LDQ = F_;
                            static constexpr int TM = D_ / 128, TN = S_ / 64, XM = 4; static constexpr bool PTR = true; };

template <int MODE> constexpr int lds_bytes() {
  return Cfg<MODE>::NS * (Cfg<MODE>::BM + Cfg<MODE>::BN) * 128 + 4096;
}

__device__ __forceinline__ void glds16(unsigned voff, const void* sbase, unsigned lds_addr) {
  asm volatile("s_mov_b32 m0, %2\n\ts_nop 0\n\tglobal_load_lds_dwordx4 %0, %1" ::"v"(voff), "s"(sbase), "s"(lds_addr) : "memory");
}
template <int OFF>
__device__ __forceinline__ bf16x4 tr_read(unsigned addr) {
  return __builtin_amdgcn_ds_read_tr16_b64_v4i16((LDS_AS bf16x4*)(size_t)(addr + OFF));
}

__device__ __forceinline__ float2 merge_row(const float2* __restrict__ part) {
  float m[NPART], q = 0.f, ms = 0.f;
#pragma unroll
  for (int i = 0; i < NPART; ++i) { float2 v = part[i]; m[i] = v.x; q += v.y; ms += v.x; }
  const float mean = ms * (1.0f / NPART);
  float dd = 0.f;
#pragma unroll
  for (int i = 0; i < NPART; ++i) { float d = m[i] - mean; dd += d * d; }
  const float var = (q + 64.0f * dd) * (1.0f / D_);
  return make_float2(mean, 1.0f / sqrtf(var + LN_EPS));
}

template <int MODE>
__global__ __launch_bounds__(512, 2) void gemm_k(const u16* __restrict__ p0, const u16* __restrict__ p1,
                                                 const u16* __restrict__ q0, const u16* __restrict__ q1,
                                                 const u16* __restrict__ xin, u16* __restrict__ out,
                                                 const float2* __restrict__ stats_in, float2* __restrict__ stats_out,
                                                 const float* __restrict__ cs) {
  using C = Cfg<MODE>;
  constexpr int BM = C::BM, BN = C::BN, NS = C::NS, NK = C::NK;
  constexpr bool PTR = C::PTR;
  constexpr bool KSPLIT = (MODE != 3);
  constexpr int NWN = KSPLIT ? 2 : 4;
  constexpr int WM = BM / 2, WN = BN / NWN, MF = WM / 16, NF = WN / 16;
  constexpr int PBYTES = BM * 128, QBYTES = BN * 128, STAGE = PBYTES + QBYTES;
  constexpr int PI = BM / 64, QI = BN / 64, PER = PI + QI;
  extern __shared__ __attribute__((aligned(16))) char lds[];
  float2* st_lds = (float2*)(lds + NS * STAGE);
  float* cs_lds = (float*)(lds + NS * STAGE + 2048);

  const int tid = threadIdx.x, lane = tid & 63;
  const int w = __builtin_amdgcn_readfirstlane(tid >> 6);
  const int kg = KSPLIT ? (w >> 2) : 0;
  const int wm = KSPLIT ? ((w >> 1) & 1) : (w >> 2), wn = KSPLIT ? (w & 1) : (w & 3);
  const int fr = lane & 15, fq = lane >> 4;
  const unsigned lds0 = (unsigned)(size_t)(LDS_AS char*)lds;

  int tm, tn;
  {
    const int b = blockIdx.x, xcd = b & 7, idx = b >> 3;
    constexpr int XM = C::XM, NXM = C::TM / XM;
    const int xm = xcd % NXM, xn = xcd / NXM;
    tm = xm * XM + idx % XM;
    tn = xn * (C::TN / (8 / NXM)) + idx / XM;
  }
  const int m0 = tm * (MODE == 3 ? 128 : BM), n0 = tn * BN;

  const u16 *pb0, *pb1, *qb0, *qb1;
  if constexpr (MODE == 1) {
    pb0 = pb1 = p0 + (size_t)m0 * C::LDP;
    qb0 = qb1 = (n0 < D_ ? q0 + (size_t)n0 * C::LDQ : q1 + (size_t)(n0 - D_) * C::LDQ);
  } else if constexpr (MODE == 2) {
    pb0 = p0 + (size_t)m0 * C::LDP; pb1 = p0 + (size_t)(D_ + m0) * C::LDP;
    qb0 = q0 + (size_t)n0 * C::LDQ; qb1 = q1 + (size_t)n0 * C::LDQ;
  } else if constexpr (MODE == 3) {
    pb0 = pb1 = ((w >> 2) ? p1 : p0) + m0;
    qb0 = qb1 = q0 + (size_t)n0 * C::LDQ;
  } else {
    pb0 = pb1 = p0 + m0;
    qb0 = qb1 = q0 + (size_t)n0 * C::LDQ;
  }

  unsigned voff_p[PI], voff_q[QI];
#pragma unroll
  for (int i = 0; i < PI; ++i) {
    if constexpr (!PTR) {
      voff_p[i] = (unsigned)(((w * PI + i) * 8 + (lane >> 3)) * C::LDP * 2 + (((lane & 7) ^ (lane >> 3)) << 4));
    } else {
      const int ii = (w * PI + i) & 15;
      const int f = ((lane >> 4) & 3) | (((ii >> 1) & 1) << 2);
      const int col = ((((lane & 15) >> 1) ^ f) << 4) + ((lane & 1) << 3);
      voff_p[i] = (unsigned)(((4 * ii + (lane >> 4)) * C::LDP + col) * 2);
    }
  }
#pragma unroll
  for (int i = 0; i < QI; ++i)
    voff_q[i] = (unsigned)(((w * QI + i) * 8 + (lane >> 3)) * C::LDQ * 2 + (((lane & 7) ^ (lane >> 3)) << 4));
  const unsigned lds_wp = lds0 + w * PI * 1024, lds_wq = lds0 + PBYTES + w * QI * 1024;

  auto issue = [&](int kt, int buf) {
    const bool seg = (MODE == 2) && (kt >= C::NKSEG);
    const int kk = (seg ? kt - C::NKSEG : kt) * 64;
    const u16* pp = (seg ? pb1 : pb0) + (PTR ? (size_t)kk * C::LDP : (size_t)kk);
    const u16* qq = (seg ? qb1 : qb0) + kk;
#pragma unroll
    for (int i = 0; i < PI; ++i) glds16(voff_p[i], pp, lds_wp + buf * STAGE + i * 1024);
#pragma unroll
    for (int i = 0; i < QI; ++i) glds16(voff_q[i], qq, lds_wq + buf * STAGE + i * 1024);
  };

  if (w < 4) __builtin_amdgcn_s_setprio(1);
  auto issue_piece = [&](int kt, int buf, int i) {
    const bool seg = (MODE == 2) && (kt >= C::NKSEG);
    const int kk = (seg ? kt - C::NKSEG : kt) * 64;
    if (i < PI) glds16(voff_p[i < PI ? i : 0], (seg ? pb1 : pb0) + (PTR ? (size_t)kk * C::LDP : (size_t)kk), lds_wp + buf * STAGE + i * 1024);
    else glds16(voff_q[i >= PI ? i - PI : 0], (seg ? qb1 : qb0) + kk, lds_wq + buf * STAGE + (i - PI) * 1024);
  };
#pragma unroll
  for (int s = 0; s < NS; ++s) issue(s, s);

  if constexpr (MODE == 1) {
    if (tid < 128) st_lds[tid] = merge_row(stats_in + (size_t)(m0 + tid) * NPART);
  } else if constexpr (MODE == 3) {
    if (tid < 256) st_lds[tid] = merge_row(stats_in + (size_t)(n0 + tid) * NPART);
    else {
      const int t2 = tid - 256, sub = t2 >> 7, c = m0 + (t2 & 127);
      float s = 0.f;
#pragma unroll
      for (int r = 0; r < CSG; ++r) s += cs[r * (2 * F_) + sub * F_ + c];
      cs_lds[t2] = s;
    }
  }

  constexpr int NEP = KSPLIT ? NF / 2 : 1;
  u16x4 xres[NEP][MF];
  if constexpr (MODE == 2 || MODE == 4) {
#pragma unroll
    for (int nj = 0; nj < NEP; ++nj) {
      const int row = n0 + wn * WN + (kg * (NF / 2) + nj) * 16 + fr;
#pragma unroll
      for (int mi = 0; mi < MF; ++mi)
        xres[nj][mi] = *(const u16x4*)(xin + (size_t)row * D_ + m0 + wm * WM + mi * 16 + fq * 4);
    }
  }

  unsigned tra[MF];
  if constexpr (PTR) {
    const int g = lane >> 4, q = (lane & 15) >> 2, pp = lane & 3;
    const int f = q | ((g & 1) << 2);
#pragma unroll
    for (int mi = 0; mi < MF; ++mi) {
      int sub, mf;
      if constexpr (MODE == 3) { sub = mi / (MF / 2); mf = wm * (MF / 2) + (mi % (MF / 2)); }
      else { sub = 0; mf = wm * MF + mi; }
      tra[mi] = lds0 + sub * 16384 + (8 * g + q) * 256 + pp * 8 + ((mf ^ f) << 5) + (KSPLIT ? kg * 8192 : 0);
    }
  }
  const int poff = (wm * WM + fr) * 128, qoff = PBYTES + (wn * WN + fr) * 128;
#define READ_SET(BUF, KH, PF, QF)                                                                           \
  do {                                                                                                      \
    const char* sb_ = lds + (BUF) * STAGE;                                                                  \
    const int sw_ = ((((KH) * 4 + fq) ^ (fr & 7)) << 4);                                                    \
    if constexpr (!PTR) {                                                                                   \
      _Pragma("unroll") for (int mi = 0; mi < MF; ++mi)                                                     \
          PF[mi] = *(const bf16x8*)(sb_ + poff + mi * 2048 + sw_);                                          \
    } else {                                                                                                \
      _Pragma("unroll") for (int mi = 0; mi < MF; ++mi) {                                                   \
        const unsigned a_ = tra[mi] + (BUF) * STAGE;                                                        \
        bf16x4 lo_ = tr_read<(KSPLIT ? 0 : (KH)) * 8192>(a_), hi_ = tr_read<(KSPLIT ? 0 : (KH)) * 8192 + 1024>(a_); \
        PF[mi] = __builtin_shufflevector(lo_, hi_, 0, 1, 2, 3, 4, 5, 6, 7);                                 \
      }                                                                                                     \
    }                                                                                                       \
    _Pragma("unroll") for (int ni = 0; ni < NF; ++ni)                                                       \
        QF[ni] = *(const bf16x8*)(sb_ + qoff + ni * 2048 + sw_);                                            \
  } while (0)

  f32x4 acc[MF][NF];
#pragma unroll
  for (int mi = 0; mi < MF; ++mi)
#pragma unroll
    for (int ni = 0; ni < NF; ++ni) acc[mi][ni] = (f32x4){0.f, 0.f, 0.f, 0.f};

#define MFMA_STMT(PF, QF) acc[mi][ni] = __builtin_amdgcn_mfma_f32_16x16x32_bf16(PF[mi], QF[ni], acc[mi][ni], 0, 0, 0)
#define MMA(PF, QF)                                                                                       \
  do {                                                                                                    \
    _Pragma("unroll") for (int mi = 0; mi < MF; ++mi) _Pragma("unroll") for (int ni = 0; ni < NF; ++ni)   \
        MFMA_STMT(PF, QF);                                                                                \
  } while (0)

  if constexpr (KSPLIT) {
    bf16x8 pA[MF], qA[NF], pB[MF], qB[NF];
    asm volatile("s_waitcnt vmcnt(%0)" ::"n"((NS - 1) * PER) : "memory");
    __builtin_amdgcn_s_barrier();
    READ_SET(0, kg, pA, qA);
    for (int kt0 = 0; kt0 < NK; kt0 += NS) {
#pragma unroll
      for (int s = 0; s < NS; ++s) {
        asm volatile("s_waitcnt vmcnt(%0) lgkmcnt(0)" ::"n"((NS - 2) * PER) : "memory");
        __builtin_amdgcn_s_barrier();
        const int kn = (kt0 + s + NS < NK) ? kt0 + s + NS : NK - 1;
        if (s & 1) READ_SET((s + 1) % NS, kg, pA, qA);
        else       READ_SET((s + 1) % NS, kg, pB, qB);
#pragma unroll
        for (int mi = 0; mi < MF; ++mi) {
#pragma unroll
          for (int ni = 0; ni < NF; ++ni) { if (s & 1) MFMA_STMT(pB, qB); else MFMA_STMT(pA, qA); }
          if (mi < PER) issue_piece(kn, s, mi);
        }
      }
    }
  } else {
    bf16x8 pA[MF], qA[NF];
    for (int kt0 = 0; kt0 < NK; kt0 += NS) {
#pragma unroll
      for (int s = 0; s < NS; ++s) {
        asm volatile("s_waitcnt vmcnt(%0) lgkmcnt(0)" ::"n"((NS - 2) * PER) : "memory");
        __builtin_amdgcn_s_barrier();
        const int kn = (kt0 + s + NS - 1 < NK) ? kt0 + s + NS - 1 : NK - 1;
#pragma unroll
        for (int kh = 0; kh < 2; ++kh) {
          const char* sb_ = lds + s * STAGE;
          const int sw_ = (((kh * 4 + fq) ^ (fr & 7)) << 4);
#pragma unroll
          for (int ni = 0; ni < NF; ++ni) qA[ni] = *(const bf16x8*)(sb_ + qoff + ni * 2048 + sw_);
#pragma unroll
          for (int mi = 0; mi < MF; ++mi) {
            const unsigned a_ = tra[mi] + s * STAGE;
            bf16x4 lo_, hi_;
            if (kh == 0) { lo_ = tr_read<0>(a_); hi_ = tr_read<1024>(a_); }
            else { lo_ = tr_read<8192>(a_); hi_ = tr_read<8192 + 1024>(a_); }
            pA[mi] = __builtin_shufflevector(lo_, hi_, 0, 1, 2, 3, 4, 5, 6, 7);
          }
#pragma unroll
          for (int mi = 0; mi < MF; ++mi) {
#pragma unroll
            for (int ni = 0; ni < NF; ++ni) MFMA_STMT(pA, qA);
            if (kh == 0 && kt0 + s > 0) issue_piece(kn, (s + NS - 1) % NS, mi);
            if (mi & 1) __builtin_amdgcn_sched_barrier(0);
          }
          __builtin_amdgcn_sched_barrier(0);
        }
      }
    }
  }
  asm volatile("s_waitcnt vmcnt(0)" ::: "memory");

  auto finish = [&](auto GC) {
    constexpr int G = decltype(GC)::value;
    int tz = threadIdx.x;
    asm volatile("" : "+v"(tz));
    const int lane = tz & 63, fr = lane & 15, fq = lane >> 4;
    constexpr int NE = KSPLIT ? NF / 2 : NF;
    constexpr int nb = KSPLIT ? G * (NF / 2) : 0;
    if constexpr (KSPLIT) {
      __builtin_amdgcn_s_barrier();
      f32x4* xch = (f32x4*)lds;
      constexpr int ob = (1 - G) * (NF / 2);
#pragma unroll
      for (int mi = 0; mi < MF; ++mi)
#pragma unroll
        for (int j = 0; j < NF / 2; ++j) xch[(w * (MF * NF / 2) + mi * (NF / 2) + j) * 64 + lane] = acc[mi][ob + j];
      __syncthreads();
      const int pw = w ^ 4;
#pragma unroll
      for (int mi = 0; mi < MF; ++mi)
#pragma unroll
        for (int j = 0; j < NF / 2; ++j) acc[mi][nb + j] += xch[(pw * (MF * NF / 2) + mi * (NF / 2) + j) * 64 + lane];
    }

    if constexpr (MODE == 1) {
#pragma unroll
      for (int mi = 0; mi < MF; ++mi) {
        const int sl = wm * WM + mi * 16 + fq * 4;
        const f32x4 st01 = *(const f32x4*)(&st_lds[sl]), st23 = *(const f32x4*)(&st_lds[sl + 2]);
#pragma unroll
        for (int nj = 0; nj < NE; ++nj) {
          const int ni = nb + nj;
          const int e = n0 + wn * WN + ni * 16 + fr;
          const float c = cs[e];
          const float sg = (e >= D_) ? -1.f : 1.f;
          u16x4 o;
          o[0] = f2bf(sg * st01[1] * (acc[mi][ni][0] - st01[0] * c));
          o[1] = f2bf(sg * st01[3] * (acc[mi][ni][1] - st01[2] * c));
          o[2] = f2bf(sg * st23[1] * (acc[mi][ni][2] - st23[0] * c));
          o[3] = f2bf(sg * st23[3] * (acc[mi][ni][3] - st23[2] * c));
          *(u16x4*)(out + (size_t)e * S_ + m0 + sl) = o;
        }
      }
    } else if constexpr (MODE == 3) {
      constexpr int HF = MF / 2;
#pragma unroll
      for (int ni = 0; ni < NF; ++ni) {
        const int rl = wn * WN + ni * 16 + fr;
        const float2 st = st_lds[rl];
#pragma unroll
        for (int mi = 0; mi < HF; ++mi) {
          const int cl = wm * (WM / 2) + mi * 16 + fq * 4;
          const f32x4 cg = *(const f32x4*)(&cs_lds[cl]), cu = *(const f32x4*)(&cs_lds[128 + cl]);
          u16x4 o;
#pragma unroll
          for (int j = 0; j < 4; ++j) {
            const float g = st.y * (acc[mi][ni][j] - st.x * cg[j]);
            const float u = st.y * (acc[HF + mi][ni][j] - st.x * cu[j]);
            o[j] = f2bf(u * __builtin_amdgcn_rcpf(1.0f + __expf(-g)));
          }
          *(u16x4*)(out + (size_t)(n0 + rl) * F_ + m0 + cl) = o;
        }
      }
    } else {
#pragma unroll
      for (int nj = 0; nj < NE; ++nj) {
        const int ni = nb + nj;
        const int row = n0 + wn * WN + ni * 16 + fr;
        float v[MF][4];
        float s = 0.f;
#pragma unroll
        for (int mi = 0; mi < MF; ++mi) {
          const int col = m0 + wm * WM + mi * 16 + fq * 4;
          const u16x4 xi = xres[nj][mi];
          u16x4 o;
#pragma unroll
          for (int j = 0; j < 4; ++j) {
            o[j] = f2bf(bf2f(xi[j]) + acc[mi][ni][j]);
            v[mi][j] = bf2f(o[j]);
            s += v[mi][j];
          }
          *(u16x4*)(out + (size_t)row * D_ + col) = o;
        }
        if (stats_out != nullptr) {
          s += __shfl_xor(s, 16); s += __shfl_xor(s, 32);
          const float mw = s * (1.0f / 64.0f);
          float q = 0.f;
#pragma unroll
          for (int mi = 0; mi < MF; ++mi)
#pragma unroll
            for (int j = 0; j < 4; ++j) { const float d = v[mi][j] - mw; q += d * d; }
          q += __shfl_xor(q, 16); q += __shfl_xor(q, 32);
          if (fq == 0) stats_out[(size_t)row * NPART + ((m0 + wm * WM) >> 6)] = make_float2(mw, q);
        }
      }
    }
  };
  if (kg == 0) finish(std::integral_constant<int, 0>{});
  else finish(std::integral_constant<int, 1>{});
}

__global__ __launch_bounds__(256) void prep_k(const u16* __restrict__ x, const u16* __restrict__ wre,
                                              const u16* __restrict__ wim, const u16* __restrict__ wg,
                                              const u16* __restrict__ wu, float2* __restrict__ stats,
                                              float* __restrict__ cs1, float* __restrict__ csp,
                                              const u16* __restrict__ cr, const u16* __restrict__ ci,
                                              const u16* __restrict__ wd) {
  const int b = blockIdx.x, tid = threadIdx.x, lane = tid & 63, w = tid >> 6;
  if (b >= 1024 + CSG * 14) {
    const int id = b - (1024 + CSG * 14);
    const size_t off = (size_t)id * 16384 + tid * 8;
    const size_t n1 = (size_t)S_ * S_;
#pragma unroll
    for (int i = 0; i < 8; ++i) {
      size_t o = off + (size_t)i * 2048;
      const u16* p = (o < n1) ? cr + o : (o < 2 * n1 ? ci + (o - n1) : wd + (o - 2 * n1));
      bf16x8 v = *(const bf16x8*)p;
      asm volatile("" ::"v"(v));
    }
    return;
  }
  if (b < 512) {
    const int row = b * 4 + w;
    const u16* p = x + (size_t)row * D_ + lane * 16;
    bf16x8 a = *(const bf16x8*)p, c = *(const bf16x8*)(p + 8);
    float v[16], s = 0.f;
#pragma unroll
    for (int j = 0; j < 8; ++j) { v[j] = bf2f((u16)a[j]); v[8 + j] = bf2f((u16)c[j]); }
#pragma unroll
    for (int j = 0; j < 16; ++j) s += v[j];
    s += __shfl_xor(s, 1); s += __shfl_xor(s, 2);
    const float mw = s * (1.0f / 64.0f);
    float q = 0.f;
#pragma unroll
    for (int j = 0; j < 16; ++j) { const float d = v[j] - mw; q += d * d; }
    q += __shfl_xor(q, 1); q += __shfl_xor(q, 2);
    if ((lane & 3) == 0) stats[(size_t)row * NPART + (lane >> 2)] = make_float2(mw, q);
  } else if (b < 1024) {
    const int row = (b - 512) * 4 + w;
    const u16* p = (row < D_ ? wre + (size_t)row * D_ : wim + (size_t)(row - D_) * D_) + lane * 16;
    bf16x8 a = *(const bf16x8*)p, c = *(const bf16x8*)(p + 8);
    float s = 0.f;
#pragma unroll
    for (int j = 0; j < 8; ++j) s += bf2f((u16)a[j]) + bf2f((u16)c[j]);
#pragma unroll
    for (int o = 1; o < 64; o <<= 1) s += __shfl_xor(s, o);
    if (lane == 0) cs1[row] = s;
  } else {
    const int id = b - 1024;
    const int rg = id / 14, cb = id % 14;
    const int sub = cb / 7, c = (cb % 7) * 512 + tid * 2;
    const u16* p = (sub ? wu : wg) + (size_t)(rg * 64) * F_ + c;
    float s0 = 0.f, s1 = 0.f;
#pragma unroll 16
    for (int r = 0; r < 64; ++r) {
      const unsigned v = *(const unsigned*)(p + (size_t)r * F_);
      s0 += __uint_as_float(v << 16); s1 += __uint_as_float(v & 0xffff0000u);
    }
    csp[rg * (2 * F_) + sub * F_ + c] = s0;
    csp[rg * (2 * F_) + sub * F_ + c + 1] = s1;
  }
}

extern "C" void kernel_launch(void* const* d_in, const int* in_sizes, int n_in, void* d_out, int out_size,
                              void* d_ws, size_t ws_size, hipStream_t stream) {
  const u16* x0 = (const u16*)d_in[0];
  const u16* cr = (const u16*)d_in[1];
  const u16* ci = (const u16*)d_in[2];
  const u16* wre = (const u16*)d_in[3];
  const u16* wim = (const u16*)d_in[4];
  const u16* wg = (const u16*)d_in[6];
  const u16* wu = (const u16*)d_in[7];
  const u16* wd = (const u16*)d_in[8];
  u16* outp = (u16*)d_out;

  char* ws = (char*)d_ws;
  u16* abT = (u16*)(ws);
  u16* xa = (u16*)(ws + (8u << 20));
  u16* xb = (u16*)(ws + (12u << 20));
  u16* h = (u16*)(ws + (16u << 20));
  float2* stats = (float2*)(ws + (32u << 20));
  float* cs1 = (float*)(ws + (32u << 20) + (512u << 10));
  float* csp = (float*)(ws + (33u << 20));

  {
    hipFuncSetAttribute((const void*)gemm_k<1>, hipFuncAttributeMaxDynamicSharedMemorySize, lds_bytes<1>());
    hipFuncSetAttribute((const void*)gemm_k<2>, hipFuncAttributeMaxDynamicSharedMemorySize, lds_bytes<2>());
    hipFuncSetAttribute((const void*)gemm_k<3>, hipFuncAttributeMaxDynamicSharedMemorySize, lds_bytes<3>());
    hipFuncSetAttribute((const void*)gemm_k<4>, hipFuncAttributeMaxDynamicSharedMemorySize, lds_bytes<4>());
  }

  prep_k<<<1024 + CSG * 14 + 704, 256, 0, stream>>>(x0, wre, wim, wg, wu, stats, cs1, csp, cr, ci, wd);
  const u16* xcur = x0;
  u16* xbuf[2] = {xa, xb};
  for (int layer = 0; layer < 2; ++layer) {
    u16* x1 = xbuf[0];
    u16* x2 = (layer == 0) ? xbuf[1] : outp;
    gemm_k<1><<<Cfg<1>::TM * Cfg<1>::TN, 512, lds_bytes<1>(), stream>>>(xcur, nullptr, wre, wim, nullptr, abT, stats, nullptr, cs1);
    gemm_k<2><<<Cfg<2>::TM * Cfg<2>::TN, 512, lds_bytes<2>(), stream>>>(abT, nullptr, cr, ci, xcur, x1, nullptr, stats, nullptr);
    gemm_k<3><<<Cfg<3>::TM * Cfg<3>::TN, 512, lds_bytes<3>(), stream>>>(wg, wu, x1, nullptr, nullptr, h, stats, nullptr, csp);
    gemm_k<4><<<Cfg<4>::TM * Cfg<4>::TN, 512, lds_bytes<4>(), stream>>>(wd, nullptr, h, nullptr, x1, x2, nullptr, layer == 0 ? stats : nullptr, nullptr);
    xcur = x2;
  }
}
```

```cpp
#include <hip/hip_runtime.h>
#include <hip/hip_bf16.h>
#include <stdint.h>
#include <type_traits>

typedef unsigned short u16;
typedef __attribute__((ext_vector_type(8))) short bf16x8;
typedef __attribute__((ext_vector_type(4))) short bf16x4;
typedef __attribute__((ext_vector_type(4))) float f32x4;
typedef __attribute__((ext_vector_type(4))) unsigned short u16x4;
#define LDS_AS __attribute__((address_space(3)))

constexpr int S_ = 2048, D_ = 1024, F_ = 3584;
constexpr int NPART = 16;
constexpr int CSG = 16;
constexpr float LN_EPS = 1e-5f;

__device__ __forceinline__ float bf2f(u16 h) { return __uint_as_float(((unsigned)h) << 16); }
__device__ __forceinline__ u16 f2bf(float f) { __hip_bfloat16 h = __float2bfloat16(f); return __builtin_bit_cast(u16, h); }

template <int MODE> struct Cfg;
template <> struct Cfg<1> { static constexpr int BM = 128, BN = 128, NS = 4, NK = 16, NKSEG = 16, LDP = D_, LDQ = D_;
                            static constexpr int TM = S_ / 128, TN = 2 * D_ / 128, XM = 4; static constexpr bool PTR = false; };
template <> struct Cfg<2> { static constexpr int BM = 128, BN = 64, NS = 4, NK = 64, NKSEG = 32, LDP = S_, LDQ = S_;
                            static constexpr int TM = D_ / 128, TN = S_ / 64, XM = 4; static constexpr bool PTR = false; };
template <> struct Cfg<3> { static constexpr int BM = 256, BN = 256, NS = 2, NK = 16, NKSEG = 16, LDP = F_, LDQ = D_;
                            static constexpr int TM = F_ / 128, TN = S_ / 256, XM = 7; static constexpr bool PTR = true; };
template <> struct Cfg<4> { static constexpr int BM = 128, BN = 64, NS = 4, NK = 56, NKSEG = 56, LDP = D_, LDQ = F_;
                            static constexpr int TM = D_ / 128, TN = S_ / 64, XM = 4; static constexpr bool PTR = true; };

template <int MODE> constexpr int lds_bytes() {
  return Cfg<MODE>::NS * (Cfg<MODE>::BM + Cfg<MODE>::BN) * 128 + 4096;
}

__device__ __forceinline__ void glds16(unsigned voff, const void* sbase, unsigned lds_addr) {
  asm volatile("s_mov_b32 m0, %2\n\ts_nop 0\n\tglobal_load_lds_dwordx4 %0, %1" ::"v"(voff), "s"(sbase), "s"(lds_addr) : "memory");
}
template <int OFF>
__device__ __forceinline__ bf16x4 tr_read(unsigned addr) {
  return __builtin_amdgcn_ds_read_tr16_b64_v4i16((LDS_AS bf16x4*)(size_t)(addr + OFF));
}

__device__ __forceinline__ float2 merge_row(const float2* __restrict__ part) {
  float m[NPART], q = 0.f, ms = 0.f;
#pragma unroll
  for (int i = 0; i < NPART; ++i) { float2 v = part[i]; m[i] = v.x; q += v.y; ms += v.x; }
  const float mean = ms * (1.0f / NPART);
  float dd = 0.f;
#pragma unroll
  for (int i = 0; i < NPART; ++i) { float d = m[i] - mean; dd += d * d; }
  const float var = (q + 64.0f * dd) * (1.0f / D_);
  return make_float2(mean, 1.0f / sqrtf(var + LN_EPS));
}

template <int MODE>
__global__ __launch_bounds__(512, 2) void gemm_k(const u16* __restrict__ p0, const u16* __restrict__ p1,
                                                 const u16* __restrict__ q0, const u16* __restrict__ q1,
                                                 const u16* __restrict__ xin, u16* __restrict__ out,
                                                 const float2* __restrict__ stats_in, float2* __restrict__ stats_out,
                                                 const float* __restrict__ cs) {
  using C = Cfg<MODE>;
  constexpr int BM = C::BM, BN = C::BN, NS = C::NS, NK = C::NK;
  constexpr bool PTR = C::PTR;
  constexpr bool KSPLIT = (MODE != 3);
  constexpr int NWN = KSPLIT ? 2 : 4;
  constexpr int WM = BM / 2, WN = BN / NWN, MF = WM / 16, NF = WN / 16;
  constexpr int PBYTES = BM * 128, QBYTES = BN * 128, STAGE = PBYTES + QBYTES;
  constexpr int PI = BM / 64, QI = BN / 64, PER = PI + QI;
  extern __shared__ __attribute__((aligned(16))) char lds[];
  float2* st_lds = (float2*)(lds + NS * STAGE);
  float* cs_lds = (float*)(lds + NS * STAGE + 2048);

  const int tid = threadIdx.x, lane = tid & 63;
  const int w = __builtin_amdgcn_readfirstlane(tid >> 6);
  const int kg = KSPLIT ? (w >> 2) : 0;
  const int wm = KSPLIT ? ((w >> 1) & 1) : (w >> 2), wn = KSPLIT ? (w & 1) : (w & 3);
  const int fr = lane & 15, fq = lane >> 4;
  const unsigned lds0 = (unsigned)(size_t)(LDS_AS char*)lds;

  int tm, tn;
  {
    const int b = blockIdx.x, xcd = b & 7, idx = b >> 3;
    constexpr int XM = C::XM, NXM = C::TM / XM;
    const int xm = xcd % NXM, xn = xcd / NXM;
    tm = xm * XM + idx % XM;
    tn = xn * (C::TN / (8 / NXM)) + idx / XM;
  }
  const int m0 = tm * (MODE == 3 ? 128 : BM), n0 = tn * BN;

  const u16 *pb0, *pb1, *qb0, *qb1;
  if constexpr (MODE == 1) {
    pb0 = pb1 = p0 + (size_t)m0 * C::LDP;
    qb0 = qb1 = (n0 < D_ ? q0 + (size_t)n0 * C::LDQ : q1 + (size_t)(n0 - D_) * C::LDQ);
  } else if constexpr (MODE == 2) {
    pb0 = p0 + (size_t)m0 * C::LDP; pb1 = p0 + (size_t)(D_ + m0) * C::LDP;
    qb0 = q0 + (size_t)n0 * C::LDQ; qb1 = q1 + (size_t)n0 * C::LDQ;
  } else if constexpr (MODE == 3) {
    pb0 = pb1 = ((w >> 2) ? p1 : p0) + m0;
    qb0 = qb1 = q0 + (size_t)n0 * C::LDQ;
  } else {
    pb0 = pb1 = p0 + m0;
    qb0 = qb1 = q0 + (size_t)n0 * C::LDQ;
  }

  unsigned voff_p[PI], voff_q[QI];
#pragma unroll
  for (int i = 0; i < PI; ++i) {
    if constexpr (!PTR) {
      voff_p[i] = (unsigned)(((w * PI + i) * 8 + (lane >> 3)) * C::LDP * 2 + (((lane & 7) ^ (lane >> 3)) << 4));
    } else {
      const int ii = (w * PI + i) & 15;
      const int f = ((lane >> 4) & 3) | (((ii >> 1) & 1) << 2);
      const int col = ((((lane & 15) >> 1) ^ f) << 4) + ((lane & 1) << 3);
      voff_p[i] = (unsigned)(((4 * ii + (lane >> 4)) * C::LDP + col) * 2);
    }
  }
#pragma unroll
  for (int i = 0; i < QI; ++i)
    voff_q[i] = (unsigned)(((w * QI + i) * 8 + (lane >> 3)) * C::LDQ * 2 + (((lane & 7) ^ (lane >> 3)) << 4));
  const unsigned lds_wp = lds0 + w * PI * 1024, lds_wq = lds0 + PBYTES + w * QI * 1024;

  auto issue = [&](int kt, int buf) {
    const bool seg = (MODE == 2) && (kt >= C::NKSEG);
    const int kk = (seg ? kt - C::NKSEG : kt) * 64;
    const u16* pp = (seg ? pb1 : pb0) + (PTR ? (size_t)kk * C::LDP : (size_t)kk);
    const u16* qq = (seg ? qb1 : qb0) + kk;
#pragma unroll
    for (int i = 0; i < PI; ++i) glds16(voff_p[i], pp, lds_wp + buf * STAGE + i * 1024);
#pragma unroll
    for (int i = 0; i < QI; ++i) glds16(voff_q[i], qq, lds_wq + buf * STAGE + i * 1024);
  };

  if (w < 4) __builtin_amdgcn_s_setprio(1);
  auto issue_piece = [&](int kt, int buf, int i) {
    const bool seg = (MODE == 2) && (kt >= C::NKSEG);
    const int kk = (seg ? kt - C::NKSEG : kt) * 64;
    if (i < PI) glds16(voff_p[i < PI ? i : 0], (seg ? pb1 : pb0) + (PTR ? (size_t)kk * C::LDP : (size_t)kk), lds_wp + buf * STAGE + i * 1024);
    else glds16(voff_q[i >= PI ? i - PI : 0], (seg ? qb1 : qb0) + kk, lds_wq + buf * STAGE + (i - PI) * 1024);
  };
#pragma unroll
  for (int s = 0; s < NS; ++s) issue(s, s);

  if constexpr (MODE == 1) {
    if (tid < 128) st_lds[tid] = merge_row(stats_in + (size_t)(m0 + tid) * NPART);
  } else if constexpr (MODE == 3) {
    if (tid < 256) st_lds[tid] = merge_row(stats_in + (size_t)(n0 + tid) * NPART);
    else {
      const int t2 = tid - 256, sub = t2 >> 7, c = m0 + (t2 & 127);
      float s = 0.f;
#pragma unroll
      for (int r = 0; r < CSG; ++r) s += cs[r * (2 * F_) + sub * F_ + c];
      cs_lds[t2] = s;
    }
  }

  constexpr int NEP = KSPLIT ? NF / 2 : 1;
  u16x4 xres[NEP][MF];
  if constexpr (MODE == 2 || MODE == 4) {
#pragma unroll
    for (int nj = 0; nj < NEP; ++nj) {
      const int row = n0 + wn * WN + (kg * (NF / 2) + nj) * 16 + fr;
#pragma unroll
      for (int mi = 0; mi < MF; ++mi)
        xres[nj][mi] = *(const u16x4*)(xin + (size_t)row * D_ + m0 + wm * WM + mi * 16 + fq * 4);
    }
  }

  unsigned tra[MF];
  if constexpr (PTR) {
    const int g = lane >> 4, q = (lane & 15) >> 2, pp = lane & 3;
    const int f = q | ((g & 1) << 2);
#pragma unroll
    for (int mi = 0; mi < MF; ++mi) {
      int sub, mf;
      if constexpr (MODE == 3) { sub = mi / (MF / 2); mf = wm * (MF / 2) + (mi % (MF / 2)); }
      else { sub = 0; mf = wm * MF + mi; }
      tra[mi] = lds0 + sub * 16384 + (8 * g + q) * 256 + pp * 8 + ((mf ^ f) << 5) + (KSPLIT ? kg * 8192 : 0);
    }
  }
  const int poff = (wm * WM + fr) * 128, qoff = PBYTES + (wn * WN + fr) * 128;
#define READ_SET(BUF, KH, PF, QF)                                                                           \
  do {                                                                                                      \
    const char* sb_ = lds + (BUF) * STAGE;                                                                  \
    const int sw_ = ((((KH) * 4 + fq) ^ (fr & 7)) << 4);                                                    \
    if constexpr (!PTR) {                                                                                   \
      _Pragma("unroll") for (int mi = 0; mi < MF; ++mi)                                                     \
          PF[mi] = *(const bf16x8*)(sb_ + poff + mi * 2048 + sw_);                                          \
    } else {                                                                                                \
      _Pragma("unroll") for (int mi = 0; mi < MF; ++mi) {                                                   \
        const unsigned a_ = tra[mi] + (BUF) * STAGE;                                                        \
        bf16x4 lo_ = tr_read<(KSPLIT ? 0 : (KH)) * 8192>(a_), hi_ = tr_read<(KSPLIT ? 0 : (KH)) * 8192 + 1024>(a_); \
        PF[mi] = __builtin_shufflevector(lo_, hi_, 0, 1, 2, 3, 4, 5, 6, 7);                                 \
      }                                                                                                     \
    }                                                                                                       \
    _Pragma("unroll") for (int ni = 0; ni < NF; ++ni)                                                       \
        QF[ni] = *(const bf16x8*)(sb_ + qoff + ni * 2048 + sw_);                                            \
  } while (0)

  f32x4 acc[MF][NF];
#pragma unroll
  for (int mi = 0; mi < MF; ++mi)
#pragma unroll
    for (int ni = 0; ni < NF; ++ni) acc[mi][ni] = (f32x4){0.f, 0.f, 0.f, 0.f};

#define MFMA_STMT(PF, QF) acc[mi][ni] = __builtin_amdgcn_mfma_f32_16x16x32_bf16(PF[mi], QF[ni], acc[mi][ni], 0, 0, 0)
#define MMA(PF, QF)                                                                                       \
  do {                                                                                                    \
    _Pragma("unroll") for (int mi = 0; mi < MF; ++mi) _Pragma("unroll") for (int ni = 0; ni < NF; ++ni)   \
        MFMA_STMT(PF, QF);                                                                                \
  } while (0)

  if constexpr (KSPLIT) {
    bf16x8 pA[MF], qA[NF], pB[MF], qB[NF];
    asm volatile("s_waitcnt vmcnt(%0)" ::"n"((NS - 1) * PER) : "memory");
    __builtin_amdgcn_s_barrier();
    READ_SET(0, kg, pA, qA);
    for (int kt0 = 0; kt0 < NK; kt0 += NS) {
#pragma unroll
      for (int s = 0; s < NS; ++s) {
        asm volatile("s_waitcnt vmcnt(%0) lgkmcnt(0)" ::"n"((NS - 2) * PER) : "memory");
        __builtin_amdgcn_s_barrier();
        const int kn = (kt0 + s + NS < NK) ? kt0 + s + NS : NK - 1;
        if (s & 1) READ_SET((s + 1) % NS, kg, pA, qA);
        else       READ_SET((s + 1) % NS, kg, pB, qB);
#pragma unroll
        for (int mi = 0; mi < MF; ++mi) {
#pragma unroll
          for (int ni = 0; ni < NF; ++ni) { if (s & 1) MFMA_STMT(pB, qB); else MFMA_STMT(pA, qA); }
          if (mi < PER) issue_piece(kn, s, mi);
        }
      }
    }
  } else {
    bf16x8 pA[MF], qA[NF];
    for (int kt0 = 0; kt0 < NK; kt0 += NS) {
#pragma unroll
      for (int s = 0; s < NS; ++s) {
        asm volatile("s_waitcnt vmcnt(%0) lgkmcnt(0)" ::"n"((NS - 2) * PER) : "memory");
        __builtin_amdgcn_s_barrier();
        const int kn = (kt0 + s + NS - 1 < NK) ? kt0 + s + NS - 1 : NK - 1;
#pragma unroll
        for (int kh = 0; kh < 2; ++kh) {
          const char* sb_ = lds + s * STAGE;
          const int sw_ = (((kh * 4 + fq) ^ (fr & 7)) << 4);
#pragma unroll
          for (int ni = 0; ni < NF; ++ni) qA[ni] = *(const bf16x8*)(sb_ + qoff + ni * 2048 + sw_);
#pragma unroll
          for (int mi = 0; mi < MF; ++mi) {
            const unsigned a_ = tra[mi] + s * STAGE;
            bf16x4 lo_, hi_;
            if (kh == 0) { lo_ = tr_read<0>(a_); hi_ = tr_read<1024>(a_); }
            else { lo_ = tr_read<8192>(a_); hi_ = tr_read<8192 + 1024>(a_); }
            pA[mi] = __builtin_shufflevector(lo_, hi_, 0, 1, 2, 3, 4, 5, 6, 7);
          }
#pragma unroll
          for (int mi = 0; mi < MF; ++mi) {
#pragma unroll
            for (int ni = 0; ni < NF; ++ni) MFMA_STMT(pA, qA);
            if (kh == 0 && kt0 + s > 0) issue_piece(kn, (s + NS - 1) % NS, mi);
            if (mi & 1) __builtin_amdgcn_sched_barrier(0);
          }
          __builtin_amdgcn_sched_barrier(0);
        }
      }
    }
  }
  asm volatile("s_waitcnt vmcnt(0)" ::: "memory");

  auto finish = [&](auto GC) {
    constexpr int G = decltype(GC)::value;
    int tz = threadIdx.x;
    asm volatile("" : "+v"(tz));
    const int lane = tz & 63, fr = lane & 15, fq = lane >> 4;
    constexpr int NE = KSPLIT ? NF / 2 : NF;
    constexpr int nb = KSPLIT ? G * (NF / 2) : 0;
    if constexpr (KSPLIT) {
      __builtin_amdgcn_s_barrier();
      f32x4* xch = (f32x4*)lds;
      constexpr int ob = (1 - G) * (NF / 2);
#pragma unroll
      for (int mi = 0; mi < MF; ++mi)
#pragma unroll
        for (int j = 0; j < NF / 2; ++j) xch[(w * (MF * NF / 2) + mi * (NF / 2) + j) * 64 + lane] = acc[mi][ob + j];
      __syncthreads();
      const int pw = w ^ 4;
#pragma unroll
      for (int mi = 0; mi < MF; ++mi)
#pragma unroll
        for (int j = 0; j < NF / 2; ++j) acc[mi][nb + j] += xch[(pw * (MF * NF / 2) + mi * (NF / 2) + j) * 64 + lane];
    }

    char* stg = lds + (KSPLIT ? 65536 : 0);
    if constexpr (!KSPLIT) __builtin_amdgcn_s_barrier();
    if constexpr (MODE == 1) {
#pragma unroll
      for (int mi = 0; mi < MF; ++mi) {
        const int sl = wm * WM + mi * 16 + fq * 4;
        const f32x4 st01 = *(const f32x4*)(&st_lds[sl]), st23 = *(const f32x4*)(&st_lds[sl + 2]);
#pragma unroll
        for (int nj = 0; nj < NE; ++nj) {
          const int ni = nb + nj;
          const int e = n0 + wn * WN + ni * 16 + fr;
          const float c = cs[e];
          const float sg = (e >= D_) ? -1.f : 1.f;
          u16x4 o;
          o[0] = f2bf(sg * st01[1] * (acc[mi][ni][0] - st01[0] * c));
          o[1] = f2bf(sg * st01[3] * (acc[mi][ni][1] - st01[2] * c));
          o[2] = f2bf(sg * st23[1] * (acc[mi][ni][2] - st23[0] * c));
          o[3] = f2bf(sg * st23[3] * (acc[mi][ni][3] - st23[2] * c));
          *(u16x4*)(stg + (wn * WN + ni * 16 + fr) * 272 + sl * 2) = o;
        }
      }
    } else if constexpr (MODE == 3) {
      constexpr int HF = MF / 2;
#pragma unroll
      for (int ni = 0; ni < NF; ++ni) {
        const int rl = wn * WN + ni * 16 + fr;
        const float2 st = st_lds[rl];
#pragma unroll
        for (int mi = 0; mi < HF; ++mi) {
          const int cl = wm * (WM / 2) + mi * 16 + fq * 4;
          const f32x4 cg = *(const f32x4*)(&cs_lds[cl]), cu = *(const f32x4*)(&cs_lds[128 + cl]);
          u16x4 o;
#pragma unroll
          for (int j = 0; j < 4; ++j) {
            const float g = st.y * (acc[mi][ni][j] - st.x * cg[j]);
            const float u = st.y * (acc[HF + mi][ni][j] - st.x * cu[j]);
            o[j] = f2bf(u * __builtin_amdgcn_rcpf(1.0f + __expf(-g)));
          }
          *(u16x4*)(stg + rl * 272 + cl * 2) = o;
        }
      }
    } else {
#pragma unroll
      for (int nj = 0; nj < NE; ++nj) {
        const int ni = nb + nj;
        const int row = n0 + wn * WN + ni * 16 + fr;
        float v[MF][4];
        float s = 0.f;
#pragma unroll
        for (int mi = 0; mi < MF; ++mi) {
          const int col = m0 + wm * WM + mi * 16 + fq * 4;
          const u16x4 xi = xres[nj][mi];
          u16x4 o;
#pragma unroll
          for (int j = 0; j < 4; ++j) {
            o[j] = f2bf(bf2f(xi[j]) + acc[mi][ni][j]);
            v[mi][j] = bf2f(o[j]);
            s += v[mi][j];
          }
          *(u16x4*)(stg + (wn * WN + ni * 16 + fr) * 272 + (wm * WM + mi * 16 + fq * 4) * 2) = o;
        }
        if (stats_out != nullptr) {
          s += __shfl_xor(s, 16); s += __shfl_xor(s, 32);
          const float mw = s * (1.0f / 64.0f);
          float q = 0.f;
#pragma unroll
          for (int mi = 0; mi < MF; ++mi)
#pragma unroll
            for (int j = 0; j < 4; ++j) { const float d = v[mi][j] - mw; q += d * d; }
          q += __shfl_xor(q, 16); q += __shfl_xor(q, 32);
          if (fq == 0) stats_out[(size_t)row * NPART + ((m0 + wm * WM) >> 6)] = make_float2(mw, q);
        }
      }
    }
  };
  if (kg == 0) finish(std::integral_constant<int, 0>{});
  else finish(std::integral_constant<int, 1>{});
  {
    __syncthreads();
    const char* stg = lds + (KSPLIT ? 65536 : 0);
    constexpr int LDO = (MODE == 1) ? S_ : (MODE == 3 ? F_ : D_);
    u16* ob = out + (size_t)n0 * LDO + m0;
#pragma unroll
    for (int i = 0; i < BN * 16 / 512; ++i) {
      const int c = tid + i * 512, r = c >> 4, ch = c & 15;
      *(bf16x8*)(ob + (size_t)r * LDO + ch * 8) = *(const bf16x8*)(stg + r * 272 + ch * 16);
    }
  }
}

__global__ __launch_bounds__(256) void prep_k(const u16* __restrict__ x, const u16* __restrict__ wre,
                                              const u16* __restrict__ wim, const u16* __restrict__ wg,
                                              const u16* __restrict__ wu, float2* __restrict__ stats,
                                              float* __restrict__ cs1, float* __restrict__ csp,
                                              const u16* __restrict__ cr, const u16* __restrict__ ci,
                                              const u16* __restrict__ wd) {
  const int b = blockIdx.x, tid = threadIdx.x, lane = tid & 63, w = tid >> 6;
  if (b >= 1024 + CSG * 14) {
    const int id = b - (1024 + CSG * 14);
    const size_t off = (size_t)id * 16384 + tid * 8;
    const size_t n1 = (size_t)S_ * S_;
#pragma unroll
    for (int i = 0; i < 8; ++i) {
      size_t o = off + (size_t)i * 2048;
      const u16* p = (o < n1) ? cr + o : (o < 2 * n1 ? ci + (o - n1) : wd + (o - 2 * n1));
      bf16x8 v = *(const bf16x8*)p;
      asm volatile("" ::"v"(v));
    }
    return;
  }
  if (b < 512) {
    const int row = b * 4 + w;
    const u16* p = x + (size_t)row * D_ + lane * 16;
    bf16x8 a = *(const bf16x8*)p, c = *(const bf16x8*)(p + 8);
    float v[16], s = 0.f;
#pragma unroll
    for (int j = 0; j < 8; ++j) { v[j] = bf2f((u16)a[j]); v[8 + j] = bf2f((u16)c[j]); }
#pragma unroll
    for (int j = 0; j < 16; ++j) s += v[j];
    s += __shfl_xor(s, 1); s += __shfl_xor(s, 2);
    const float mw = s * (1.0f / 64.0f);
    float q = 0.f;
#pragma unroll
    for (int j = 0; j < 16; ++j) { const float d = v[j] - mw; q += d * d; }
    q += __shfl_xor(q, 1); q += __shfl_xor(q, 2);
    if ((lane & 3) == 0) stats[(size_t)row * NPART + (lane >> 2)] = make_float2(mw, q);
  } else if (b < 1024) {
    const int row = (b - 512) * 4 + w;
    const u16* p = (row < D_ ? wre + (size_t)row * D_ : wim + (size_t)(row - D_) * D_) + lane * 16;
    bf16x8 a = *(const bf16x8*)p, c = *(const bf16x8*)(p + 8);
    float s = 0.f;
#pragma unroll
    for (int j = 0; j < 8; ++j) s += bf2f((u16)a[j]) + bf2f((u16)c[j]);
#pragma unroll
    for (int o = 1; o < 64; o <<= 1) s += __shfl_xor(s, o);
    if (lane == 0) cs1[row] = s;
  } else {
    const int id = b - 1024;
    const int rg = id / 14, cb = id % 14;
    const int sub = cb / 7, c = (cb % 7) * 512 + tid * 2;
    const u16* p = (sub ? wu : wg) + (size_t)(rg * 64) * F_ + c;
    float s0 = 0.f, s1 = 0.f;
#pragma unroll 16
    for (int r = 0; r < 64; ++r) {
      const unsigned v = *(const unsigned*)(p + (size_t)r * F_);
      s0 += __uint_as_float(v << 16); s1 += __uint_as_float(v & 0xffff0000u);
    }
    csp[rg * (2 * F_) + sub * F_ + c] = s0;
    csp[rg * (2 * F_) + sub * F_ + c + 1] = s1;
  }
}

extern "C" void kernel_launch(void* const* d_in, const int* in_sizes, int n_in, void* d_out, int out_size,
                              void* d_ws, size_t ws_size, hipStream_t stream) {
  const u16* x0 = (const u16*)d_in[0];
  const u16* cr = (const u16*)d_in[1];
  const u16* ci = (const u16*)d_in[2];
  const u16* wre = (const u16*)d_in[3];
  const u16* wim = (const u16*)d_in[4];
  const u16* wg = (const u16*)d_in[6];
  const u16* wu = (const u16*)d_in[7];
  const u16* wd = (const u16*)d_in[8];
  u16* outp = (u16*)d_out;

  char* ws = (char*)d_ws;
  u16* abT = (u16*)(ws);
  u16* xa = (u16*)(ws + (8u << 20));
  u16* xb = (u16*)(ws + (12u << 20));
  u16* h = (u16*)(ws + (16u << 20));
  float2* stats = (float2*)(ws + (32u << 20));
  float* cs1 = (float*)(ws + (32u << 20) + (512u << 10));
  float* csp = (float*)(ws + (33u << 20));

  {
    hipFuncSetAttribute((const void*)gemm_k<1>, hipFuncAttributeMaxDynamicSharedMemorySize, lds_bytes<1>());
    hipFuncSetAttribute((const void*)gemm_k<2>, hipFuncAttributeMaxDynamicSharedMemorySize, lds_bytes<2>());
    hipFuncSetAttribute((const void*)gemm_k<3>, hipFuncAttributeMaxDynamicSharedMemorySize, lds_bytes<3>());
    hipFuncSetAttribute((const void*)gemm_k<4>, hipFuncAttributeMaxDynamicSharedMemorySize, lds_bytes<4>());
  }

  prep_k<<<1024 + CSG * 14 + 736, 256, 0, stream>>>(x0, wre, wim, wg, wu, stats, cs1, csp, cr, ci, wd);
  const u16* xcur = x0;
  u16* xbuf[2] = {xa, xb};
  for (int layer = 0; layer < 2; ++layer) {
    u16* x1 = xbuf[0];
    u16* x2 = (layer == 0) ? xbuf[1] : outp;
    gemm_k<1><<<Cfg<1>::TM * Cfg<1>::TN, 512, lds_bytes<1>(), stream>>>(xcur, nullptr, wre, wim, nullptr, abT, stats, nullptr, cs1);
    gemm_k<2><<<Cfg<2>::TM * Cfg<2>::TN, 512, lds_bytes<2>(), stream>>>(abT, nullptr, cr, ci, xcur, x1, nullptr, stats, nullptr);
    gemm_k<3><<<Cfg<3>::TM * Cfg<3>::TN, 512, lds_bytes<3>(), stream>>>(wg, wu, x1, nullptr, nullptr, h, stats, nullptr, csp);
    gemm_k<4><<<Cfg<4>::TM * Cfg<4>::TN, 512, lds_bytes<4>(), stream>>>(wd, nullptr, h, nullptr, x1, x2, nullptr, layer == 0 ? stats : nullptr, nullptr);
    xcur = x2;
  }
}
```
